```python
import torch

def _softmax(x, axis=-1):
    return torch.softmax(x, dim=axis)

def _gelu(x):
    return torch.nn.functional.gelu(x, approximate='tanh')

def _silu(x):
    return torch.nn.functional.silu(x)

def _sigmoid(x):
    return torch.sigmoid(x)

def _layernorm(x, axis=-1, eps=1e-05):
    m = x.mean(dim=axis, keepdim=True)
    v = x.var(dim=axis, keepdim=True, unbiased=False)
    return (x - m) / torch.sqrt(v + eps)

def _scatter_add(tgt, idx, src, axis=0):
    return tgt.clone().index_add_(axis, idx, src)

def _segment_sum(src, seg, n):
    out = torch.zeros((n,) + src.shape[1:], dtype=src.dtype)
    return out.index_add_(0, seg, src)

def _pad(x, pw):
    flat = tuple((v for pair in reversed(pw) for v in pair))
    return torch.nn.functional.pad(x, flat)
kernel_name = 'adaLN_cond__grouped_kv_mqa__mixer_channel_mlp__prompt'

def setup_inputs():

    _w = lambda a: (lambda t: t.to(torch.bfloat16) if t.is_floating_point() else t)(torch.as_tensor(a))

    def _A_setup():
        B, S, D, H = (1, 2048, 640, 32)
        Dh = D // H
        return {'x': _w((lambda *s: torch.randn(*s, dtype=torch.bfloat16) * 0.05)(B, S, D)), 'wq': _w((lambda *s: torch.randn(*s, dtype=torch.bfloat16) * 0.05)(D, H * Dh)), 'wk': _w((lambda *s: torch.randn(*s, dtype=torch.bfloat16) * 0.05)(D, Dh)), 'wv': _w((lambda *s: torch.randn(*s, dtype=torch.bfloat16) * 0.05)(D, Dh)), 'wo': _w((lambda *s: torch.randn(*s, dtype=torch.bfloat16) * 0.05)(H * Dh, D))}
    _A_in = _A_setup()

    def _B_setup():
        B, S, D, F = (1, 2048, 640, 1728)
        return {'x': _w((lambda *s: torch.randn(*s, dtype=torch.bfloat16) * 0.05)(B, S, D)), 'w1': _w((lambda *s: torch.randn(*s, dtype=torch.bfloat16) * 3.2)(D, F) * D ** (-0.5)), 'b1': _w((lambda *s: torch.randn(*s, dtype=torch.bfloat16) * 0.8)(F)), 'w2': _w((lambda *s: torch.randn(*s, dtype=torch.bfloat16) * 3.2)(F, D) * F ** (-0.5)), 'b2': _w((lambda *s: torch.randn(*s, dtype=torch.bfloat16) * 3.2)(D))}
    _B_in = _B_setup()
    return {'A_x': _A_in['x'], 'A_wq': _A_in['wq'], 'A_wk': _A_in['wk'], 'A_wv': _A_in['wv'], 'A_wo': _A_in['wo'], 'B_x': _B_in['x'], 'B_w1': _B_in['w1'], 'B_b1': _B_in['b1'], 'B_w2': _B_in['w2'], 'B_b2': _B_in['b2'], 'c': _w((lambda *s: torch.randn(*s, dtype=torch.bfloat16) * 0.1)(1, 640) * 8.0), 'w_mod': _w((lambda *s: torch.randn(*s, dtype=torch.bfloat16) * 0.1)(640, 6 * 640) * 640 ** (-0.5) * 8.0)}

def reference(A_x, A_wq, A_wk, A_wv, A_wo, B_x, B_w1, B_b1, B_w2, B_b2, c, w_mod):

    def _A_ref(x, wq, wk, wv, wo):
        B, S, D = x.shape
        H = 32
        Dh = D // H
        q = (lambda x, *d: x.permute(*d))((x @ wq).reshape(B, S, H, Dh), 0, 2, 1, 3)
        k = x @ wk
        v = x @ wv
        scores = torch.einsum('bhqd,bkd->bhqk', q, k) * Dh ** (-0.5)
        p = _softmax(scores, axis=-1)
        o = torch.einsum('bhqk,bkd->bhqd', p, v)
        return (lambda x, *d: x.permute(*d))(o, 0, 2, 1, 3).reshape(B, S, H * Dh) @ wo

    def _B_ref(x, w1, b1, w2, b2):
        y = _layernorm(x, axis=-1)
        h = _gelu(torch.einsum('bsd,df->bsf', y, w1) + b1)
        return torch.einsum('bsf,fd->bsd', h, w2) + b2
    x = A_x
    mod = (_silu(c) @ w_mod).reshape(c.shape[0], 6, 1, -1)
    sa, ca, ga, sb, cb, gb = (mod[:, 0].clone(), mod[:, 1].clone(), mod[:, 2].clone(), mod[:, 3].clone(), mod[:, 4].clone(), mod[:, 5].clone())
    h = _layernorm(x, axis=-1) * (1 + ca) + sa
    x = x + ga * _A_ref(h, A_wq, A_wk, A_wv, A_wo)
    h = _layernorm(x, axis=-1) * (1 + cb) + sb
    x = x + gb * _B_ref(h, B_w1, B_b1, B_w2, B_b2)
    return x
```

```cpp
#include <hip/hip_runtime.h>
#include <hip/hip_bf16.h>
typedef __hip_bfloat16 bf16;
constexpr int S = 2048, D = 640, H = 32, DH = 20, F = 1728;
__device__ __forceinline__ float b2f(bf16 v) { return __bfloat162float(v); }

__global__ void k_mod(const bf16* c, const bf16* w, float* mod) {
    int n = blockIdx.x * 64 + threadIdx.x;
    float acc = 0;
    for (int k = 0; k < D; ++k) { float cv = b2f(c[k]); float s = cv / (1.f + __expf(-cv)); acc += s * b2f(w[k * 3840 + n]); }
    mod[n] = acc;
}
__global__ void k_ln(const bf16* xb, const float* xf, const float* shift, const float* scale, int second, float* out) {
    int row = blockIdx.x, l = threadIdx.x;
    float v[10]; float s = 0;
    for (int i = 0; i < 10; ++i) { v[i] = xb ? b2f(xb[row * D + i * 64 + l]) : xf[row * D + i * 64 + l]; s += v[i]; }
    for (int o = 32; o; o >>= 1) s += __shfl_xor(s, o);
    float m = s / D, q = 0;
    for (int i = 0; i < 10; ++i) { v[i] -= m; q += v[i] * v[i]; }
    for (int o = 32; o; o >>= 1) q += __shfl_xor(q, o);
    float r = rsqrtf(q / D + 1e-5f);
    for (int i = 0; i < 10; ++i) v[i] = v[i] * r * (1.f + scale[i * 64 + l]) + shift[i * 64 + l];
    if (second) {
        s = 0; for (int i = 0; i < 10; ++i) s += v[i];
        for (int o = 32; o; o >>= 1) s += __shfl_xor(s, o);
        m = s / D; q = 0;
        for (int i = 0; i < 10; ++i) { v[i] -= m; q += v[i] * v[i]; }
        for (int o = 32; o; o >>= 1) q += __shfl_xor(q, o);
        r = rsqrtf(q / D + 1e-5f);
        for (int i = 0; i < 10; ++i) v[i] *= r;
    }
    for (int i = 0; i < 10; ++i) out[row * D + i * 64 + l] = v[i];
}
__global__ void k_gemm(const float* A, const bf16* W, int K, int N, int mode, const bf16* xb, const float* xf, const float* g, const bf16* bias, float* C, bf16* Cb) {
    int n = blockIdx.x * 64 + threadIdx.x, m0 = blockIdx.y * 8;
    if (n >= N) return;
    float acc[8] = {};
    for (int k = 0; k < K; ++k) { float w = b2f(W[k * N + n]);
#pragma unroll
        for (int i = 0; i < 8; ++i) acc[i] += A[(m0 + i) * K + k] * w; }
#pragma unroll
    for (int i = 0; i < 8; ++i) { int m = m0 + i; float a = acc[i];
        if (mode == 0) C[m * N + n] = a;
        else if (mode == 1) C[m * N + n] = b2f(xb[m * N + n]) + g[n] * a;
        else if (mode == 2) { a += b2f(bias[n]); float t = 0.7978845608f * (a + 0.044715f * a * a * a); C[m * N + n] = 0.5f * a * (1.f + tanhf(t)); }
        else Cb[m * N + n] = __float2bfloat16(xf[m * N + n] + g[n] * (a + b2f(bias[n]))); }
}
__global__ void k_attn(const float* q, const float* k, const float* v, float* o) {
    int idx = blockIdx.x * 64 + threadIdx.x; int row = idx / H, h = idx % H;
    float qq[DH], acc[DH]; float m = -1e30f, l = 0;
#pragma unroll
    for (int d = 0; d < DH; ++d) { qq[d] = q[row * D + h * DH + d] * 0.2236067977f; acc[d] = 0; }
    for (int j = 0; j < S; ++j) { float s = 0;
#pragma unroll
        for (int d = 0; d < DH; ++d) s += qq[d] * k[j * DH + d];
        float mn = fmaxf(m, s), a = __expf(m - mn), p = __expf(s - mn); l = l * a + p;
#pragma unroll
        for (int d = 0; d < DH; ++d) acc[d] = acc[d] * a + p * v[j * DH + d];
        m = mn; }
#pragma unroll
    for (int d = 0; d < DH; ++d) o[row * D + h * DH + d] = acc[d] / l;
}
extern "C" void kernel_launch(void* const* d_in, const int* in_sizes, int n_in, void* d_out, int out_size, void* d_ws, size_t ws_size, hipStream_t stream) {
    const bf16* x = (const bf16*)d_in[0]; const bf16* wq = (const bf16*)d_in[1]; const bf16* wk = (const bf16*)d_in[2]; const bf16* wv = (const bf16*)d_in[3];
    const bf16* wo = (const bf16*)d_in[4]; const bf16* w1 = (const bf16*)d_in[6]; const bf16* b1 = (const bf16*)d_in[7]; const bf16* w2 = (const bf16*)d_in[8];
    const bf16* b2 = (const bf16*)d_in[9]; const bf16* c = (const bf16*)d_in[10]; const bf16* wm = (const bf16*)d_in[11];
    float* ws = (float*)d_ws; float* mod = ws; float* h = ws + 4096; float* q = h + S * D; float* k = q + S * D; float* v = k + S * DH; float* o = v + S * DH; float* x1 = o + S * D; float* hh = x1 + S * D;
    k_mod<<<60, 64, 0, stream>>>(c, wm, mod);
    k_ln<<<S, 64, 0, stream>>>(x, nullptr, mod, mod + D, 0, h);
    k_gemm<<<dim3(10, S / 8), 64, 0, stream>>>(h, wq, D, D, 0, nullptr, nullptr, nullptr, nullptr, q, nullptr);
    k_gemm<<<dim3(1, S / 8), 64, 0, stream>>>(h, wk, D, DH, 0, nullptr, nullptr, nullptr, nullptr, k, nullptr);
    k_gemm<<<dim3(1, S / 8), 64, 0, stream>>>(h, wv, D, DH, 0, nullptr, nullptr, nullptr, nullptr, v, nullptr);
    k_attn<<<S * H / 64, 64, 0, stream>>>(q, k, v, o);
    k_gemm<<<dim3(10, S / 8), 64, 0, stream>>>(o, wo, D, D, 1, x, nullptr, mod + 2 * D, nullptr, x1, nullptr);
    k_ln<<<S, 64, 0, stream>>>(nullptr, x1, mod + 3 * D, mod + 4 * D, 1, h);
    k_gemm<<<dim3(27, S / 8), 64, 0, stream>>>(h, w1, D, F, 2, nullptr, nullptr, nullptr, b1, hh, nullptr);
    k_gemm<<<dim3(10, S / 8), 64, 0, stream>>>(hh, w2, F, D, 3, nullptr, x1, mod + 5 * D, b2, nullptr, (bf16*)d_out);
}
```

```cpp
#include <hip/hip_runtime.h>
#include <hip/hip_bf16.h>
typedef unsigned u32; typedef unsigned long long u64; typedef unsigned short u16;
typedef u32 v4u __attribute__((ext_vector_type(4)));
typedef u32 v2u __attribute__((ext_vector_type(2)));
typedef float f32x2 __attribute__((ext_vector_type(2)));
typedef float f32x4 __attribute__((ext_vector_type(4)));
typedef float f32x16 __attribute__((ext_vector_type(16)));
typedef __bf16 bf16x8 __attribute__((ext_vector_type(8)));
typedef __bf16 bf16x2 __attribute__((ext_vector_type(2)));
typedef short s16x4 __attribute__((ext_vector_type(4)));
#define GAS __attribute__((address_space(1)))
#define LAS __attribute__((address_space(3)))
#define RLX_AGENT __ATOMIC_RELAXED, __HIP_MEMORY_SCOPE_AGENT
#define DI __device__ __forceinline__
#define SB() __builtin_amdgcn_sched_barrier(0)

constexpr u32 WS_E = 0, WS_MODG = 1024, WS_KVF = 16384, WS_WF = 18432, WS_GF = 32768, WS_KV = 131072, WS_O = 327680,
              WS_XG = WS_O + 32 * 81920, WS_ST = WS_XG + 32 * 81920, WS_H = WS_ST + 32 * 8 * 64 * 64, WS_W1F = WS_H + 32 * 221184,
              WS_W2F = WS_W1F + 2160 * 1024, WS_END = WS_W2F + 2160 * 1024, WS_DBG = 20u << 20;
constexpr u32 KVC = 11264;
constexpr u32 L_WQ = 0, L_WKV = 102400, L_H = 0, L_KVRED = 83968, L_PP = 128256, L_WO = 0, L_RING = 102400, L_WRED = 102400,
              L_X = 0, L_STP = 122880, L_PART = 0,
              L_G1 = 147456, L_SA = 150016, L_AUX = 152576, L_MRED = 156416, L_USW = 156928, L_ABCD = 160000, L_OWN = 161024, L_TOTAL = 163840;
constexpr int HS = 1296;
constexpr u32 SPIN_CAP = 400000;

DI float bflo(u32 v) { return __uint_as_float(v << 16); }
DI float bfhi(u32 v) { return __uint_as_float(v & 0xffff0000u); }
DI u32 pk2(float a, float b) { f32x2 v = {a, b}; return __builtin_bit_cast(u32, __builtin_convertvector(v, bf16x2)); }
DI float bfr(float a) { return bflo(pk2(a, a)); }
DI f32x16 mfma32(v4u a, v4u b, f32x16 c) { return __builtin_amdgcn_mfma_f32_32x32x16_bf16(__builtin_bit_cast(bf16x8, a), __builtin_bit_cast(bf16x8, b), c, 0, 0, 0); }
DI f32x4 mfma16(v4u a, v4u b, f32x4 c) { return __builtin_amdgcn_mfma_f32_16x16x32_bf16(__builtin_bit_cast(bf16x8, a), __builtin_bit_cast(bf16x8, b), c, 0, 0, 0); }
DI void bar() { asm volatile("s_waitcnt lgkmcnt(0)" ::: "memory"); __builtin_amdgcn_s_barrier(); asm volatile("" ::: "memory"); }
DI void vm0() { asm volatile("s_waitcnt vmcnt(0)" ::: "memory"); }
template <int CTRL> DI u32 dppu(u32 v) { return (u32)__builtin_amdgcn_update_dpp(0, (int)v, CTRL, 0xf, 0xf, true); }
template <int CTRL> DI float dpp(float v) { return __uint_as_float(dppu<CTRL>(__float_as_uint(v))); }
constexpr int X1 = 0xB1, X2 = 0x4E, HM = 0x141, R4 = 0x124, R8 = 0x128;
DI float t16(float a, float b) { auto r = __builtin_amdgcn_permlane16_swap(__float_as_uint(a), __float_as_uint(b), false, false); return __uint_as_float(r[0]) + __uint_as_float(r[1]); }
DI float t32(float a, float b) { auto r = __builtin_amdgcn_permlane32_swap(__float_as_uint(a), __float_as_uint(b), false, false); return __uint_as_float(r[0]) + __uint_as_float(r[1]); }
DI float hi32(float a) { auto r = __builtin_amdgcn_permlane32_swap(__float_as_uint(a), __float_as_uint(a), false, false); return __uint_as_float(r[1]); }
DI float sum16(float v) { v += dpp<X1>(v); v += dpp<X2>(v); v += dpp<R4>(v); v += dpp<R8>(v); return v; }
DI u32 umax16(u32 v) { v = max(v, dppu<X1>(v)); v = max(v, dppu<X2>(v)); v = max(v, dppu<R4>(v)); v = max(v, dppu<R8>(v)); return v; }
DI u32 umax64(u32 v) { v = umax16(v); auto r = __builtin_amdgcn_permlane16_swap(v, v, false, false); v = max(r[0], r[1]); auto q = __builtin_amdgcn_permlane32_swap(v, v, false, false); return max(q[0], q[1]); }

struct Args { const char *x, *wq, *wk, *wv, *wo, *w1, *b1, *w2, *b2, *c, *wmod; char* out; char* ws; };

__global__ __launch_bounds__(512, 2) void fused_block(Args a) {
    extern __shared__ __attribute__((aligned(16))) char lds_[];
    LAS char* lds = (LAS char*)lds_;
    const int t = threadIdx.x, lane = t & 63, w = __builtin_amdgcn_readfirstlane(t >> 6);
    const int b = blockIdx.x, xcd = b & 7, j = (b >> 3) & 7, grp = xcd * 4 + (b >> 6), row0 = grp * 64, mi = b >> 3;
    const int hi = lane >> 5, g = lane >> 4, l15 = lane & 15, l31 = lane & 31, q4 = (lane & 15) >> 2, p4 = lane & 3;
    GAS char* ws = (GAS char*)a.ws;
    const __amdgpu_buffer_rsrc_t rs = __builtin_amdgcn_make_buffer_rsrc(a.ws, 0, WS_END, 0x00020000);
#define LD16(off) (*(const LAS v4u*)(lds + (off)))
#define LD8(off) (*(const LAS v2u*)(lds + (off)))
#define LDF4(off) (*(const LAS f32x4*)(lds + (off)))
#define ST16(off, v) (*(LAS v4u*)(lds + (off)) = (v))
#define ST8(off, v) (*(LAS v2u*)(lds + (off)) = (v))
#define STF4(off, v) (*(LAS f32x4*)(lds + (off)) = (v))
#define STF(off, v) (*(LAS float*)(lds + (off)) = (v))
#define LDF(off) (*(const LAS float*)(lds + (off)))
#define TR(off) __builtin_bit_cast(v2u, __builtin_amdgcn_ds_read_tr16_b64_v4i16((LAS s16x4*)(lds + (off))))
#define GL16(p) (*(const GAS v4u*)(p))
#define GL8(p) (*(const GAS v2u*)(p))
#define GLDS(p, off) __builtin_amdgcn_global_load_lds((const GAS u32*)(p), (LAS u32*)(lds + (off)), 16, 0, 0)
#define CLD16(off) __builtin_amdgcn_raw_buffer_load_b128(rs, (off), 0, 16)
#define CLD8(off) __builtin_amdgcn_raw_buffer_load_b64(rs, (off), 0, 16)
#define CST16(off, v) __builtin_amdgcn_raw_buffer_store_b128((v), rs, (off), 0, 16)
#define CST8(off, v) __builtin_amdgcn_raw_buffer_store_b64((v), rs, (off), 0, 16)
#define TS(i)
    auto tr2 = [&](u32 o0, u32 o1) { v2u x0 = TR(o0), x1 = TR(o1); return (v4u){x0.x, x0.y, x1.x, x1.y}; };
    TS(0);
    const u32 E = __hip_atomic_load((GAS u32*)(ws + WS_E), RLX_AGENT);
    const bool modblk = mi < 30;
    const int mcol = mi < 10 ? xcd * 160 + mi * 16 : 1280 + xcd * 320 + (mi - 10) * 16;
    v4u wm[3]; u16 cc[3];
    {
        const int mr = t >> 1, r2 = min(mr + 512, 639);
        const GAS char* wp = (const GAS char*)a.wmod + (modblk ? mcol : 0) * 2 + (t & 1) * 16;
        if (modblk) { wm[0] = GL16(wp + mr * 7680); wm[1] = GL16(wp + (mr + 256) * 7680); wm[2] = GL16(wp + r2 * 7680);
            const GAS u16* cp = (const GAS u16*)a.c; cc[0] = cp[mr]; cc[1] = cp[mr + 256]; cc[2] = cp[r2]; }
    }
    SB();
    v4u xr[10];
    {
        const GAS char* xp = (const GAS char*)a.x + (size_t)(row0 + 8 * w + (lane >> 3)) * 1280 + (lane & 7) * 16;
#pragma unroll
        for (int i = 0; i < 10; ++i) xr[i] = GL16(xp + i * 128);
    }
    SB();
    const int tq = j >> 1, isV = j & 1;
    {
        const GAS char* kp = (const GAS char*)(isV ? a.wv : a.wk) + lane * 16;
#pragma unroll
        for (int i = 0; i < 3; ++i) GLDS(kp + (w + 8 * i) * 1024, L_WKV + (w + 8 * i) * 1024);
        if (w == 0) GLDS(kp + 24 * 1024, L_WKV + 24 * 1024);
    }
    auto slice_glds = [&](const char* W, u32 dst) {
        const __amdgpu_buffer_rsrc_t wr = __builtin_amdgcn_make_buffer_rsrc((void*)W, 0, 640 * 1280, 0x00020000);
        const u32 r = (u32)lane / 10u, vo = r * 1280 + (lane - r * 10) * 16;
#pragma unroll
        for (int k = 0; k < 14; ++k) { const int i = w + 8 * k;
            if (k < 13 || i < 106 || (i == 106 && lane < 40)) __builtin_amdgcn_raw_ptr_buffer_load_lds(wr, (LAS u32*)(lds + dst + i * 960), 16, vo, i * 7680 + j * 160, 0, 0); }
    };
    slice_glds(a.wq, L_WQ);
    SB();
    const u32 tag = E * 1664525u + 1013904223u;
    if (modblk) {
        float acc[8] = {};
#pragma unroll
        for (int i = 0; i < 3; ++i) {
            const float cv = bflo(cc[i]); float s = bfr(cv / (1.f + __expf(-cv)));
            if (i == 2 && t >= 256) s = 0.f;
#pragma unroll
            for (int e = 0; e < 4; ++e) { acc[2 * e] += s * bflo(wm[i][e]); acc[2 * e + 1] += s * bfhi(wm[i][e]); }
        }
#pragma unroll
        for (int i = 0; i < 4; ++i) acc[i] = t32(acc[i], acc[i + 4]);
#pragma unroll
        for (int i = 0; i < 2; ++i) { acc[i] = t16(acc[i], acc[i + 2]); acc[i] += dpp<X2>(acc[i]); acc[i] += dpp<R4>(acc[i]); acc[i] += dpp<R8>(acc[i]); }
        if ((lane & 14) == 0) *(LAS f32x2*)(lds + L_MRED + (w * 16 + 8 * (lane & 1) + 4 * hi + 2 * (g & 1)) * 4) = (f32x2){acc[0], acc[1]};
        bar();
        if (t < 8) { float s0 = 0, s1 = 0;
#pragma unroll
            for (int i = 0; i < 8; ++i) { s0 += LDF(L_MRED + (i * 16 + 2 * t) * 4); s1 += LDF(L_MRED + (i * 16 + 2 * t + 1) * 4); }
            CST8(WS_MODG + (mcol / 2 + t) * 8, ((v2u){pk2(s0, s1), tag})); }
    }
    TS(1);
    {
        float s4[4] = {};
#pragma unroll
        for (int i = 0; i < 10; ++i)
#pragma unroll
            for (int e = 0; e < 4; ++e) s4[e] += bflo(xr[i][e]) + bfhi(xr[i][e]);
        float s = (s4[0] + s4[1]) + (s4[2] + s4[3]);
        s += dpp<X1>(s); s += dpp<X2>(s); s += dpp<HM>(s);
        const float m = s * (1.f / 640.f); float v4[4] = {};
#pragma unroll
        for (int i = 0; i < 10; ++i)
#pragma unroll
            for (int e = 0; e < 4; ++e) { const float d0 = bflo(xr[i][e]) - m, d1 = bfhi(xr[i][e]) - m; v4[e] += d0 * d0; v4[e] += d1 * d1; }
        float v = (v4[0] + v4[1]) + (v4[2] + v4[3]);
        v += dpp<X1>(v); v += dpp<X2>(v); v += dpp<HM>(v);
        const float mb = bfr(m), rsd = 1.f / bfr(sqrtf(bfr(bfr(v * (1.f / 640.f)) + 1e-5f))), nb = -mb * rsd;
#pragma unroll
        for (int i = 0; i < 10; ++i) {
#pragma unroll
            for (int e = 0; e < 4; ++e) xr[i][e] = pk2(bflo(xr[i][e]) * rsd + nb, bfhi(xr[i][e]) * rsd + nb);
            asm volatile("" : "+v"(xr[i])); }
    }
    TS(2);
    vm0(); bar();
    TS(3);
    const int hh = w & 3, th = w >> 2;
    v4u wf[40];
    {
        const u32 base = L_WQ + (8 * hi + q4) * 160 + (20 * hh + 16 * (g & 1) + 4 * p4) * 2;
#pragma unroll
        for (int ks = 0; ks < 40; ++ks) wf[ks] = tr2(base + ks * 2560, base + ks * 2560 + 640);
    }
    auto gather4 = [&](int col, bool act) {
        v4u v;
        for (u32 sp = 0; sp < SPIN_CAP; ++sp) { v = CLD16(WS_MODG + col * 4); if (__all(!act || (v.y == tag && v.w == tag))) break; __builtin_amdgcn_s_sleep(1); }
        return (v2u){v.x, v.z};
    };
    if (w < 5) { const v2u v = gather4(4 * t, true); f32x4 f = {bflo(v.x), bfhi(v.x), bflo(v.y), bfhi(v.y)};
        if (t >= 160) { f.x = bfr(1.f + f.x); f.y = bfr(1.f + f.y); f.z = bfr(1.f + f.z); f.w = bfr(1.f + f.w); STF4(L_G1 + (t - 160) * 16, f); } else STF4(L_SA + t * 16, f); }
    bar();
    TS(4);
    {
        const u32 hb = L_H + (8 * w + (lane >> 3)) * HS + (lane & 7) * 16, mb = (lane & 7) * 32;
#pragma unroll
        for (int i = 0; i < 10; ++i) {
            const f32x4 g0 = LDF4(L_G1 + mb + i * 256), g1 = LDF4(L_G1 + mb + i * 256 + 16), s0 = LDF4(L_SA + mb + i * 256), s1 = LDF4(L_SA + mb + i * 256 + 16);
            v4u o;
            o.x = pk2(bflo(xr[i].x) * g0.x + s0.x, bfhi(xr[i].x) * g0.y + s0.y); o.y = pk2(bflo(xr[i].y) * g0.z + s0.z, bfhi(xr[i].y) * g0.w + s0.w);
            o.z = pk2(bflo(xr[i].z) * g1.x + s1.x, bfhi(xr[i].z) * g1.y + s1.y); o.w = pk2(bflo(xr[i].w) * g1.z + s1.z, bfhi(xr[i].w) * g1.w + s1.w);
            ST16(hb + i * 128, o);
        }
    }
    auto pp_item = [&](int n, bool& isw2, int& ks, int& nt) { int i = 17 * b + n; isw2 = i >= 2160; if (isw2) { i -= 2160; ks = i / 40; nt = i - ks * 40; } else { ks = i / 54; nt = i - ks * 54; } return i < 2160; };
#pragma unroll
    for (int r = 0; r < 3; ++r) { const int n = w + 8 * r; bool isw2; int ks, nt;
        if (n < 17 && pp_item(n, isw2, ks, nt)) {
            const GAS char* src;
            if (isw2) src = (const GAS char*)a.w2 + (32 * ks + (lane >> 1)) * 1280 + nt * 32 + (lane & 1) * 16;
            else { const int jj = ks / 5, ii = ks - jj * 5, lr = lane >> 2; const int gr = ii < 4 ? 80 * jj + 32 * (ii >> 1) + 8 * (ii & 1) + 16 * (lr >> 3) + (lr & 7) : 80 * jj + 64 + lr;
                src = (const GAS char*)a.w1 + gr * 3456 + nt * 64 + p4 * 16; }
            GLDS(src, L_PP + n * 1024);
        } }
    bar();
    TS(5);
    {
        const int nt = w & 1, kq = w >> 1; f32x4 acc = {};
#pragma unroll
        for (int i = 0; i < 5; ++i) { const int ks = 5 * kq + i;
            const v4u hf = LD16(L_H + (16 * tq + l15) * HS + ks * 64 + g * 16);
            const u32 wb = L_WKV + (32 * ks + 8 * g + q4) * 40 + nt * 32 + p4 * 8; const v4u kf = tr2(wb, wb + 160);
            acc = isV ? mfma16(hf, kf, acc) : mfma16(kf, hf, acc); }
        STF4(L_KVRED + w * 1024 + lane * 16, acc);
    }
    bar();
    if (w == 0) {
        f32x4 s0 = {}, s1 = {};
#pragma unroll
        for (int kq = 0; kq < 4; ++kq) { s0 += LDF4(L_KVRED + (2 * kq) * 1024 + lane * 16); s1 += LDF4(L_KVRED + (2 * kq + 1) * 1024 + lane * 16); }
        const v2u p0 = {pk2(s0.x, s0.y), pk2(s0.z, s0.w)}; v2u p1 = {pk2(s1.x, s1.y), pk2(s1.z, s1.w)};
        const int key0 = row0 + 16 * tq; const u32 cb = WS_KV + (key0 >> 7) * KVC, tile = (key0 >> 5) & 3; float n2 = 0;
        if (isV) {
            const u32 o = cb + 4096 + tile * 1536 + (tq & 1) * 768 + (g & 1) * 384 + l15 * 16 + 8 * (g >> 1);
            CST8(o, p0);
            if (l15 >= 4) { const u32 c = l15 == 4 ? 0x3F803F80u : 0u; p1 = (v2u){c, c}; }
            if (l15 < 8) CST8(o + 256, p1);
        } else {
            const int kk = (key0 & 31) + l15;
            CST8(cb + tile * 1024 + (kk + 32 * (g & 1)) * 16 + 8 * (g >> 1), p0);
            if (g == 0) CST8(cb + 10240 + tile * 256 + kk * 8, p1);
            n2 = bflo(p0.x) * bflo(p0.x) + bfhi(p0.x) * bfhi(p0.x) + bflo(p0.y) * bflo(p0.y) + bfhi(p0.y) * bfhi(p0.y);
            if (g == 0) n2 += bflo(p1.x) * bflo(p1.x) + bfhi(p1.x) * bfhi(p1.x) + bflo(p1.y) * bflo(p1.y) + bfhi(p1.y) * bfhi(p1.y);
            n2 = t16(n2, n2); n2 = t32(n2, n2);
            const u32 nb = umax16(__float_as_uint(n2));
            n2 = __uint_as_float(nb);
        }
        vm0();
        if (lane == 0) CST8(WS_KVF + b * 8, ((v2u){__float_as_uint(n2), tag}));
    }
    TS(6);
    v4u qf0, qf1; float qn2;
    {
        f32x16 acc = {};
        const u32 hb = L_H + (32 * th + l31) * HS + hi * 16;
#pragma unroll
        for (int ks = 0; ks < 40; ++ks) acc = mfma32(wf[ks], LD16(hb + ks * 32), acc);
        const float sc = 0.22360679774997896f * 1.4426950408889634f;
        qf0 = (v4u){pk2(acc[0] * sc, acc[1] * sc), pk2(acc[2] * sc, acc[3] * sc), pk2(acc[4] * sc, acc[5] * sc), pk2(acc[6] * sc, acc[7] * sc)};
        qf1 = (v4u){hi ? 0u : pk2(acc[8] * sc, acc[9] * sc), hi ? 0u : pk2(acc[10] * sc, acc[11] * sc), 0u, 0u};
        qn2 = 0;
#pragma unroll
        for (int e = 0; e < 4; ++e) qn2 += bflo(qf0[e]) * bflo(qf0[e]) + bfhi(qf0[e]) * bfhi(qf0[e]);
#pragma unroll
        for (int e = 0; e < 2; ++e) qn2 += bflo(qf1[e]) * bflo(qf1[e]) + bfhi(qf1[e]) * bfhi(qf1[e]);
        qn2 = t32(qn2, qn2);
    }
    TS(7);
    vm0();
#pragma unroll
    for (int r = 0; r < 3; ++r) { const int n = w + 8 * r; bool isw2; int ks, nt;
        if (n < 17 && pp_item(n, isw2, ks, nt)) {
            const u32 o = L_PP + n * 1024 + (isw2 ? (16 * (g >> 1) + 4 * (g & 1) + q4) * 32 + p4 * 8 : (4 * hi + q4) * 64 + (g & 1) * 32 + p4 * 8);
            const v4u f = tr2(o, o + (isw2 ? 256 : 512));
            CST16((isw2 ? WS_W2F + (nt * 54 + ks) * 1024 : WS_W1F + (nt * 40 + ks) * 1024) + lane * 16, f);
        } }
    if (w < 6) {
        const bool act = t < 360; const int u = t - 320;
        const int col = t < 320 ? 1920 + 4 * t : (u < 20 ? 1280 + 80 * j + 4 * u : 3200 + 80 * j + 4 * (min(u, 39) - 20));
        v2u v = gather4(col, act); f32x4 f = {bflo(v.x), bfhi(v.x), bflo(v.y), bfhi(v.y)};
        if (t < 320) {
            const int isg = t >= 160, c = 4 * (t - 160 * isg), jj = c / 80, r = c - jj * 80;
            if (isg) { f.x = bfr(1.f + f.x); f.y = bfr(1.f + f.y); f.z = bfr(1.f + f.z); f.w = bfr(1.f + f.w); v = (v2u){pk2(f.x, f.y), pk2(f.z, f.w)}; }
            const int gg = (r >> 2) & 3, ks = r < 64 ? 5 * jj + 2 * (r >> 5) + (gg >> 1) : 5 * jj + 4, h2 = gg & 1, tt = r < 64 ? (r >> 4) & 1 : (r >> 3) & 1;
            ST8(L_AUX + (isg ? 0 : 1280) + (ks * 2 + h2) * 16 + tt * 8, v);
            if (jj == j) STF4(L_OWN + (isg ? 320 : 640) + r * 4, f);
        } else if (act) STF4(L_OWN + (u < 20 ? u * 16 : 960 + (u - 20) * 16), f);
    } else if (t < 464) ST16(L_AUX + 2560 + (t - 384) * 16, ((v4u){0x3F803F80u, 0x3F803F80u, 0x3F803F80u, 0x3F803F80u}));
    TS(8);
    float kmax2;
    {
        v4u f0, f1;
        for (u32 sp = 0; sp < SPIN_CAP; ++sp) { f0 = CLD16(WS_KVF + lane * 16); f1 = CLD16(WS_KVF + 1024 + lane * 16);
            if (__all(f0.y == tag && f0.w == tag && f1.y == tag && f1.w == tag)) break; __builtin_amdgcn_s_sleep(1); }
        const u32 nb = umax64(max(max(f0.x, f0.z), max(f1.x, f1.z)));
        kmax2 = __uint_as_float(nb);
    }
    TS(9);
    if (b == 0 && t == 0) __hip_atomic_store((GAS u32*)(ws + WS_E), tag, RLX_AGENT);
    v4u st0, st1 = {};
    auto kv_load = [&](int c) { st0 = CLD16(WS_KV + c * KVC + t * 16); if (w < 3) st1 = CLD16(WS_KV + c * KVC + 8192 + t * 16); };
    auto kv_write = [&](u32 slot) { ST16(slot + t * 16, st0); if (w < 3) ST16(slot + 8192 + t * 16, st1); };
    {
        v4u a0 = CLD16(WS_KV + t * 16), a1 = {}; if (w < 3) a1 = CLD16(WS_KV + 8192 + t * 16);
        kv_load(1);
        ST16(L_RING + t * 16, a0); if (w < 3) ST16(L_RING + 8192 + t * 16, a1);
        kv_write(L_RING + KVC);
        kv_load(2);
    }
    const float Msh = bfr(sqrtf(qn2 * kmax2) * 1.01f + 1e-30f);
    qf1.z = hi ? 0u : (pk2(-Msh, 0.f) & 0xffffu);
    asm volatile("s_waitcnt vmcnt(2)" ::: "memory");
    bar();
    if (t == 0) CST8(WS_WF + b * 8, ((v2u){0u, tag}));
    slice_glds(a.wo, L_WO);
    TS(10);
    f32x16 oacc = {};
    {
        const u32 ak0 = lane * 16, ak1 = 10240 + l31 * 8, av = 4096 + (hi * 24 + (l31 < 24 ? l31 : (l31 < 28 ? l31 - 4 : l31 - 12))) * 16;
        const u32 ONE2 = 0x3F803F80u;
        u32 cur = L_RING, nxt = L_RING + KVC, fre = L_RING + 2 * KVC;
        v4u k0 = LD16(cur + ak0); v2u k1 = LD8(cur + ak1);
        f32x16 sc = mfma32(k0, qf0, (f32x16){}); sc = mfma32((v4u){k1.x, k1.y, ONE2, ONE2}, qf1, sc);
        k0 = LD16(cur + ak0 + 1024); k1 = LD8(cur + ak1 + 256);
        v4u p1 = {}, v1 = {};
#define EX(i) __builtin_amdgcn_exp2f(sc[i])
        for (int c = 0; c < 16; ++c) {
#pragma unroll
            for (int i = 0; i < 4; ++i) {
                v4u p0; f32x16 sn;
                const v4u v0 = LD16(cur + av + i * 1536);
                const u32 kb = i < 2 ? cur + (i + 2) * 1024 : nxt + (i - 2) * 1024, kb1 = i < 2 ? cur + (i + 2) * 256 : nxt + (i - 2) * 256;
                SB();
                sn = mfma32(k0, qf0, (f32x16){});
                k0 = LD16(kb + ak0);
                p0.x = pk2(EX(0), EX(1)); p0.y = pk2(EX(2), EX(3));
                SB();
                oacc = mfma32(v1, p1, oacc);
                v1 = LD16(cur + av + i * 1536 + 768);
                p0.z = pk2(EX(4), EX(5)); p0.w = pk2(EX(6), EX(7));
                SB();
                sn = mfma32((v4u){k1.x, k1.y, ONE2, ONE2}, qf1, sn);
                k1 = LD8(kb1 + ak1);
                p1.x = pk2(EX(8), EX(9)); p1.y = pk2(EX(10), EX(11));
                SB();
                oacc = mfma32(v0, p0, oacc);
                p1.z = pk2(EX(12), EX(13)); p1.w = pk2(EX(14), EX(15));
                if (i == 0) { if (c < 14) kv_write(fre); }
                if (i == 1) { if (c < 13) kv_load(c + 3); }
                SB();
                sc = sn;
            }
            bar();
            const u32 tmp = cur; cur = nxt; nxt = fre; fre = tmp;
        }
        oacc = mfma32(v1, p1, oacc);
    }
    TS(11);
    float lsum = hi32(oacc[8]);
    if (__any(!(lsum >= 1e-12f && lsum < 3e38f))) {
        qf1.z = 0u; const u32 ONE2 = 0x3F803F80u;
        const u32 ak0 = WS_KV + lane * 16, ak1 = WS_KV + 10240 + l31 * 8, av = WS_KV + 4096 + (hi * 24 + (l31 < 24 ? l31 : 20)) * 16;
        float mx = -__builtin_inff();
        for (int tI = 0; tI < 64; ++tI) { const u32 cb = (tI >> 2) * KVC, i = tI & 3;
            const v4u k0 = CLD16(cb + ak0 + i * 1024); const v2u k1 = CLD8(cb + ak1 + i * 256);
            f32x16 s = mfma32(k0, qf0, (f32x16){}); s = mfma32((v4u){k1.x, k1.y, ONE2, ONE2}, qf1, s);
#pragma unroll
            for (int r = 0; r < 16; ++r) mx = fmaxf(mx, s[r]); }
        mx = fmaxf(mx, __shfl_xor(mx, 32));
        oacc = (f32x16){};
        for (int tI = 0; tI < 64; ++tI) { const u32 cb = (tI >> 2) * KVC, i = tI & 3;
            const v4u k0 = CLD16(cb + ak0 + i * 1024); const v2u k1 = CLD8(cb + ak1 + i * 256);
            const v4u v0 = CLD16(cb + av + i * 1536), v1 = CLD16(cb + av + i * 1536 + 768);
            f32x16 s = mfma32(k0, qf0, (f32x16){}); s = mfma32((v4u){k1.x, k1.y, ONE2, ONE2}, qf1, s);
            v4u p0, p1;
#pragma unroll
            for (int e = 0; e < 4; ++e) { p0[e] = pk2(__builtin_amdgcn_exp2f(s[2 * e] - mx), __builtin_amdgcn_exp2f(s[2 * e + 1] - mx));
                                          p1[e] = pk2(__builtin_amdgcn_exp2f(s[8 + 2 * e] - mx), __builtin_amdgcn_exp2f(s[9 + 2 * e] - mx)); }
            oacc = mfma32(v0, p0, oacc); oacc = mfma32(v1, p1, oacc); }
        lsum = hi32(oacc[8]);
    }
    const u32 gfl = WS_GF + grp * 1024;
    {
        const float il = 1.f / lsum; const int mt = 2 * th + (l31 >> 4);
        const v4u o0 = {pk2(oacc[0] * il, oacc[1] * il), pk2(oacc[2] * il, oacc[3] * il), pk2(oacc[4] * il, oacc[5] * il), pk2(oacc[6] * il, oacc[7] * il)};
        const u32 ob = WS_O + grp * 81920 + mt * 20480 + l15 * 16;
        CST16(ob + (2 * j + (hh >> 1)) * 1024 + (2 * (hh & 1) + hi) * 256, o0);
        const int ci = 64 + 2 * j + (hh >> 1);
        if (!hi) CST8(ob + ci * 256 + 8 * (hh & 1), ((v2u){pk2(oacc[8] * il, oacc[9] * il), pk2(oacc[10] * il, oacc[11] * il)}));
    }
    vm0(); bar();
    if (t == 0) __hip_atomic_store((GAS u32*)(ws + gfl + j * 128), tag, RLX_AGENT);
    TS(12);
    const int ntile = j < 6 ? 7 : 6, nt0 = j < 6 ? 7 * j : 42 + 6 * (j - 6); const bool has_tile = w < ntile;
    {
        for (u32 sp = 0; sp < SPIN_CAP; ++sp) { const v4u f0 = CLD16(WS_WF + lane * 16), f1 = CLD16(WS_WF + 1024 + lane * 16);
            if (__all(f0.y == tag && f0.w == tag && f1.y == tag && f1.w == tag)) break; __builtin_amdgcn_s_sleep(1); }
    }
    const int mp = w >> 1, par = w & 1;
    v2u xres[3];
    {
        const GAS char* xp = (const GAS char*)a.x + (size_t)(row0 + 16 * mp + l15) * 1280 + j * 160 + g * 8;
        xres[0] = GL8(xp + par * 64); xres[1] = GL8(xp + par * 64 + 32); xres[2] = GL8(xp + 128);
    }
    auto wait_flags = [&](int phase, int first, int n) {
        for (u32 sp = 0; sp < SPIN_CAP; ++sp) { const u32 f = __builtin_amdgcn_raw_buffer_load_b32(rs, gfl + phase * 32768 + (first + (lane & (n - 1))) * 128, 0, 16);
            if (__all(f == tag)) break; __builtin_amdgcn_s_sleep(1); } };
    wait_flags(0, 4 * par, 4);
    TS(13);
    u32 x1p[6]; v4u w1f[40];
    {
        v4u xf[10];
        const u32 ob = WS_O + grp * 81920 + mp * 20480 + lane * 16;
#pragma unroll
        for (int i = 0; i < 10; ++i) xf[i] = CLD16(ob + (i < 8 ? 8 * par + i : 16 + 2 * par + (i - 8)) * 1024);
        SB();
        if (has_tile) {
#pragma unroll
            for (int ks = 0; ks < 40; ++ks) w1f[ks] = CLD16(WS_W1F + ((nt0 + w) * 40 + ks) * 1024 + lane * 16);
        }
        SB();
        f32x4 acc[5] = {};
        const u32 wm_ = L_WO + (320 * par + 20 * (g >> 1) + 4 * (g & 1) + q4) * 160 + p4 * 8, wl_ = L_WO + (320 * par + 40 * g + 16 + q4) * 160 + p4 * 8;
#pragma unroll
        for (int i = 0; i < 10; ++i)
#pragma unroll
            for (int nt = 0; nt < 5; ++nt) {
                const u32 o = i < 8 ? wm_ + i * 6400 + nt * 32 : wl_ + (i - 8) * 25600 + nt * 32;
                acc[nt] = mfma16(tr2(o, o + (i < 8 ? 1280 : 3200)), xf[i], acc[nt]); }
        const u32 rb = L_WRED + w * 3072 + lane * 16;
        STF4(rb, par ? acc[0] : acc[2]); STF4(rb + 1024, par ? acc[1] : acc[3]); STF4(rb + 2048, acc[4]);
        bar();
        const u32 pb = L_WRED + (w ^ 1) * 3072 + lane * 16;
        f32x4 r[3]; r[0] = (par ? acc[2] : acc[0]) + LDF4(pb); r[1] = (par ? acc[3] : acc[1]) + LDF4(pb + 1024); r[2] = acc[4] + LDF4(pb + 2048);
        float sm[6] = {}; u32 xg[6];
#pragma unroll
        for (int i = 0; i < 3; ++i) {
            const u32 co = (i < 2 ? par * 128 + i * 64 : 256) + g * 16;
            const f32x4 ga = LDF4(L_OWN + co), g2 = LDF4(L_OWN + 320 + co), sb = LDF4(L_OWN + 640 + co);
            const float xin[4] = {bflo(xres[i].x), bfhi(xres[i].x), bflo(xres[i].y), bfhi(xres[i].y)};
            float x1[4], y[4];
#pragma unroll
            for (int e = 0; e < 4; ++e) x1[e] = xin[e] + bfr(bfr(r[i][e]) * ga[e]);
            x1p[2 * i] = pk2(x1[0], x1[1]); x1p[2 * i + 1] = pk2(x1[2], x1[3]);
            x1[0] = bflo(x1p[2 * i]); x1[1] = bfhi(x1p[2 * i]); x1[2] = bflo(x1p[2 * i + 1]); x1[3] = bfhi(x1p[2 * i + 1]);
            xg[2 * i] = pk2(x1[0] * g2[0], x1[1] * g2[1]); xg[2 * i + 1] = pk2(x1[2] * g2[2], x1[3] * g2[3]);
            y[0] = bflo(xg[2 * i]); y[1] = bfhi(xg[2 * i]); y[2] = bflo(xg[2 * i + 1]); y[3] = bfhi(xg[2 * i + 1]);
            const float on = (i < 2 || hi == par) ? 1.f : 0.f;
#pragma unroll
            for (int e = 0; e < 4; ++e) { const float xa = x1[e] * on, ya = y[e] * on;
                sm[0] += xa; sm[1] += xa * xa; sm[2] += ya; sm[3] += ya * ya; sm[4] += ya * g2[e]; sm[5] += ya * sb[e]; }
        }
        const u32 xb = WS_XG + grp * 81920 + (mp >> 1) * 40960 + (16 * (mp & 1) + l15 + 32 * (g & 1)) * 16;
        CST16(xb + (5 * j + 2 * par + (g >> 1)) * 1024, ((v4u){xg[0], xg[1], xg[2], xg[3]}));
        if (hi == par) CST8(xb + (5 * j + 4) * 1024 + 8 * (g >> 1), ((v2u){xg[4], xg[5]}));
#pragma unroll
        for (int i = 0; i < 6; ++i) { sm[i] = t16(sm[i], sm[i]); sm[i] = t32(sm[i], sm[i]); }
        const u32 sbo = WS_ST + ((grp * 8 + j) * 64 + 16 * mp + l15) * 64 + par * 32;
        if (g < 2) CST16(sbo + g * 16, g ? ((v4u){__float_as_uint(sm[4]), __float_as_uint(sm[5]), 0u, 0u}) : ((v4u){__float_as_uint(sm[0]), __float_as_uint(sm[1]), __float_as_uint(sm[2]), __float_as_uint(sm[3])}));
    }
    vm0(); bar();
    if (t == 0) __hip_atomic_store((GAS u32*)(ws + gfl + 32768 + j * 128), tag, RLX_AGENT);
    TS(14);
    float cst[5];
    if (has_tile) {
        f32x16 acc = {};
        const u32 ab = L_AUX + min(l31, 2) * 1280 + hi * 16;
#pragma unroll
        for (int ks = 0; ks < 40; ++ks) acc = mfma32(w1f[ks], LD16(ab + ks * 32), acc);
        if (l31 < 3) {
#pragma unroll
            for (int i = 0; i < 4; ++i) STF4(L_USW + w * 384 + l31 * 128 + i * 32 + hi * 16, ((f32x4){acc[4 * i], acc[4 * i + 1], acc[4 * i + 2], acc[4 * i + 3]})); }
    }
    if (w == 7) {
#pragma unroll
        for (int i = 0; i < 5; ++i) cst[i] = 0;
#pragma unroll
        for (int r = 0; r < 2; ++r) { const int ch = lane + 64 * r; const float on = ch < 80 ? 1.f : 0.f; const v4u gv = LD16(L_AUX + min(ch, 79) * 16), sv = LD16(L_AUX + 1280 + min(ch, 79) * 16);
#pragma unroll
            for (int e = 0; e < 4; ++e) { const float g0 = bflo(gv[e]) * on, g1 = bfhi(gv[e]) * on, s0 = bflo(sv[e]) * on, s1 = bfhi(sv[e]) * on;
                cst[0] += g0 + g1; cst[1] += g0 * g0 + g1 * g1; cst[2] += g0 * s0 + g1 * s1; cst[3] += s0 + s1; cst[4] += s0 * s0 + s1 * s1; } }
#pragma unroll
        for (int i = 0; i < 5; ++i) { cst[i] = sum16(cst[i]); cst[i] = t16(cst[i], cst[i]); cst[i] = t32(cst[i], cst[i]); }
    }
    wait_flags(1, w, 1);
    TS(15);
    {
        v4u xi[10]; const u32 xb = WS_XG + grp * 81920 + 5 * w * 1024 + lane * 16;
#pragma unroll
        for (int i = 0; i < 10; ++i) xi[i] = CLD16(xb + (i / 5) * 40960 + (i % 5) * 1024);
        const u32 sbo = WS_ST + ((grp * 8 + w) * 64 + lane) * 64;
        const v4u s0 = CLD16(sbo), s1 = CLD16(sbo + 16), s2 = CLD16(sbo + 32), s3 = CLD16(sbo + 48);
#pragma unroll
        for (int i = 0; i < 10; ++i) ST16(L_X + (i / 5) * 40960 + (5 * w + i % 5) * 1024 + lane * 16, xi[i]);
        STF4(L_STP + w * 2048 + lane * 16, __builtin_bit_cast(f32x4, s0) + __builtin_bit_cast(f32x4, s2));
        STF4(L_STP + w * 2048 + 1024 + lane * 16, __builtin_bit_cast(f32x4, s1) + __builtin_bit_cast(f32x4, s3));
    }
    bar();
    TS(16);
    const int ks0 = w < 6 ? 7 * w : 42 + 6 * (w - 6), nks = w < 6 ? 7 : 6;
    v4u w2f[7][5];
    {
        f32x16 a0 = {}, a1 = {};
        if (has_tile) {
#pragma unroll
            for (int ks = 0; ks < 40; ++ks) { a0 = mfma32(w1f[ks], LD16(L_X + ks * 1024 + lane * 16), a0); a1 = mfma32(w1f[ks], LD16(L_X + 40960 + ks * 1024 + lane * 16), a1); }
        }
        if (w == 7) {
            f32x4 u0 = {}, u1 = {};
#pragma unroll
            for (int i = 0; i < 8; ++i) { u0 += LDF4(L_STP + i * 2048 + lane * 16); u1 += LDF4(L_STP + i * 2048 + 1024 + lane * 16); }
            const float in = 1.f / 640.f, m1 = u0.x * in, v1 = fmaxf(u0.y * in - m1 * m1, 0.f), m1b = bfr(m1), r1 = 1.f / bfr(sqrtf(bfr(bfr(v1) + 1e-5f)));
            const float m2 = (r1 * (u0.z - m1b * cst[0]) + cst[3]) * in;
            const float q2 = (r1 * r1 * (u0.w - 2.f * m1b * u1.x + m1b * m1b * cst[1]) + 2.f * r1 * (u1.y - m1b * cst[2]) + cst[4]) * in;
            const float v2 = fmaxf(q2 - m2 * m2, 0.f), m2b = bfr(m2), r2 = 1.f / bfr(sqrtf(bfr(bfr(v2) + 1e-5f)));
            STF4(L_ABCD + lane * 16, ((f32x4){r1 * r2, -r1 * r2 * m1b, r2, -r2 * m2b}));
        }
        SB();
#pragma unroll
        for (int i = 0; i < 7; ++i)
#pragma unroll
            for (int nt = 0; nt < 5; ++nt) w2f[i][nt] = CLD16(WS_W2F + ((5 * j + nt) * 54 + ks0 + min(i, nks - 1)) * 1024 + lane * 16);
        bar();
        TS(17);
        if (has_tile) {
            const f32x4 c0 = LDF4(L_ABCD + l31 * 16), c1 = LDF4(L_ABCD + 512 + l31 * 16);
            u32 h0[8], h1[8];
#pragma unroll
            for (int i = 0; i < 4; ++i) {
                const u32 uo = L_USW + w * 384 + i * 32 + hi * 16; const f32x4 uu = LDF4(uo), ss = LDF4(uo + 128), ww = LDF4(uo + 256);
                const v2u bb = GL8((const GAS char*)a.b1 + ((nt0 + w) * 32 + 8 * i + 4 * hi) * 2);
                const float b1v[4] = {bflo(bb.x), bfhi(bb.x), bflo(bb.y), bfhi(bb.y)};
                float y0[4], y1[4];
#pragma unroll
                for (int e = 0; e < 4; ++e) {
                    const float z0 = bfr(bfr(c0.x * a0[4 * i + e] + c0.y * uu[e] + c0.z * ss[e] + c0.w * ww[e]) + b1v[e]);
                    const float z1 = bfr(bfr(c1.x * a1[4 * i + e] + c1.y * uu[e] + c1.z * ss[e] + c1.w * ww[e]) + b1v[e]);
                    const float k1 = -2.f * 0.7978845608028654f * 1.4426950408889634f, k3 = k1 * 0.044715f;
                    y0[e] = z0 * __builtin_amdgcn_rcpf(1.f + __builtin_amdgcn_exp2f(z0 * (k1 + k3 * z0 * z0)));
                    y1[e] = z1 * __builtin_amdgcn_rcpf(1.f + __builtin_amdgcn_exp2f(z1 * (k1 + k3 * z1 * z1)));
                }
                h0[2 * i] = pk2(y0[0], y0[1]); h0[2 * i + 1] = pk2(y0[2], y0[3]); h1[2 * i] = pk2(y1[0], y1[1]); h1[2 * i + 1] = pk2(y1[2], y1[3]);
            }
            const u32 hb = WS_H + grp * 221184 + ((l31 >> 4) * 54 + nt0 + w) * 1024 + (l15 + 16 * hi) * 16;
            CST16(hb, ((v4u){h0[0], h0[1], h0[2], h0[3]})); CST16(hb + 512, ((v4u){h0[4], h0[5], h0[6], h0[7]}));
            CST16(hb + 108 * 1024, ((v4u){h1[0], h1[1], h1[2], h1[3]})); CST16(hb + 108 * 1024 + 512, ((v4u){h1[4], h1[5], h1[6], h1[7]}));
        }
    }
    vm0(); bar();
    if (t == 0) __hip_atomic_store((GAS u32*)(ws + gfl + 65536 + j * 128), tag, RLX_AGENT);
    TS(18);
    wait_flags(2, w, 1);
    TS(19);
    {
        f32x4 own[3];
        const u32 hb = WS_H + grp * 221184 + ks0 * 1024 + lane * 16;
#pragma unroll
        for (int half = 0; half < 2; ++half) {
            v4u hf[2][7];
#pragma unroll
            for (int m = 0; m < 2; ++m) { const int mt = (mp + 1 + 2 * half + m) & 3;
#pragma unroll
                for (int i = 0; i < 7; ++i) hf[m][i] = CLD16(hb + (mt * 54 + min(i, nks - 1)) * 1024); }
#pragma unroll
            for (int m = 0; m < 2; ++m) { const int mt = (mp + 1 + 2 * half + m) & 3;
                f32x4 acc[5] = {};
                if (nks < 7) hf[m][6] = (v4u){};
#pragma unroll
                for (int i = 0; i < 7; ++i)
#pragma unroll
                    for (int nt = 0; nt < 5; ++nt) acc[nt] = mfma16(w2f[i][nt], hf[m][i], acc[nt]);
                if (half == 1 && m == 1) {
                    const u32 pb = L_PART + mt * 35840 + 6 * 5120 + lane * 16;
                    STF4(pb + (par ? 0 : 2048), par ? acc[0] : acc[2]); STF4(pb + (par ? 1024 : 3072), par ? acc[1] : acc[3]);
                    if (hi != par) STF4(pb + 4096, acc[4]);
                    own[0] = par ? acc[2] : acc[0]; own[1] = par ? acc[3] : acc[1]; own[2] = acc[4];
                } else {
                    const u32 pb = L_PART + mt * 35840 + ((w - 2 * mt - 2) & 7) * 5120 + lane * 16;
#pragma unroll
                    for (int nt = 0; nt < 5; ++nt) STF4(pb + nt * 1024, acc[nt]);
                }
            }
        }
        bar();
        TS(20);
        GAS char* op = (GAS char*)a.out + (size_t)(row0 + 16 * mp + l15) * 1280 + j * 160 + g * 8;
#pragma unroll
        for (int i = 0; i < 3; ++i) {
            const u32 pb = L_PART + mp * 35840 + (i < 2 ? 2 * par + i : 4) * 1024 + lane * 16; f32x4 s = own[i];
#pragma unroll
            for (int k = 0; k < 7; ++k) s += LDF4(pb + k * 5120);
            const int co = (i < 2 ? par * 32 + i * 16 : 64) + g * 4;
            const f32x4 gb = LDF4(L_OWN + 960 + co * 4); const v2u bb = GL8((const GAS char*)a.b2 + (80 * j + co) * 2);
            const float o0 = bflo(x1p[2 * i]) + bfr(bfr(bfr(s.x) + bflo(bb.x)) * gb.x), o1 = bfhi(x1p[2 * i]) + bfr(bfr(bfr(s.y) + bfhi(bb.x)) * gb.y);
            const float o2 = bflo(x1p[2 * i + 1]) + bfr(bfr(bfr(s.z) + bflo(bb.y)) * gb.z), o3 = bfhi(x1p[2 * i + 1]) + bfr(bfr(bfr(s.w) + bfhi(bb.y)) * gb.w);
            if (i < 2 || hi == par) *(GAS v2u*)(op + (i < 2 ? par * 64 + i * 32 : 128)) = (v2u){pk2(o0, o1), pk2(o2, o3)};
        }
    }
    TS(21);
}

extern "C" void kernel_launch(void* const* d_in, const int* in_sizes, int n_in, void* d_out, int out_size, void* d_ws, size_t ws_size, hipStream_t stream) {
    Args a;
    a.x = (const char*)d_in[0]; a.wq = (const char*)d_in[1]; a.wk = (const char*)d_in[2]; a.wv = (const char*)d_in[3]; a.wo = (const char*)d_in[4];
    a.w1 = (const char*)d_in[6]; a.b1 = (const char*)d_in[7]; a.w2 = (const char*)d_in[8]; a.b2 = (const char*)d_in[9]; a.c = (const char*)d_in[10]; a.wmod = (const char*)d_in[11];
    a.out = (char*)d_out; a.ws = (char*)d_ws;
    (void)hipFuncSetAttribute((const void*)fused_block, hipFuncAttributeMaxDynamicSharedMemorySize, L_TOTAL);
    hipLaunchKernelGGL(fused_block, dim3(256), dim3(512), L_TOTAL, stream, a);
}
```

```cpp
#include <hip/hip_runtime.h>
#include <hip/hip_bf16.h>
typedef unsigned u32; typedef unsigned long long u64; typedef unsigned short u16;
typedef u32 v4u __attribute__((ext_vector_type(4)));
typedef u32 v2u __attribute__((ext_vector_type(2)));
typedef float f32x2 __attribute__((ext_vector_type(2)));
typedef float f32x4 __attribute__((ext_vector_type(4)));
typedef float f32x16 __attribute__((ext_vector_type(16)));
typedef __bf16 bf16x8 __attribute__((ext_vector_type(8)));
typedef __bf16 bf16x2 __attribute__((ext_vector_type(2)));
typedef short s16x4 __attribute__((ext_vector_type(4)));
#define GAS __attribute__((address_space(1)))
#define LAS __attribute__((address_space(3)))
#define RLX_AGENT __ATOMIC_RELAXED, __HIP_MEMORY_SCOPE_AGENT
#define DI __device__ __forceinline__
#define SB() __builtin_amdgcn_sched_barrier(0)

constexpr u32 KVC = 12288;
constexpr u32 WS_E = 0, WS_MODG = 1024, WS_KVF = 16384, WS_WF = 18432, WS_GF = 32768, WS_KV = 163840, WS_O = WS_KV + 16 * KVC,
              WS_XG = WS_O + 32 * 81920, WS_ST = WS_XG + 32 * 81920, WS_H = WS_ST + 32 * 8 * 64 * 64, WS_W1F = WS_H + 32 * 221184,
              WS_W2F = WS_W1F + 2160 * 1024, WS_END = WS_W2F + 2160 * 1024, WS_DBG = 20u << 20;
constexpr u32 L_H = 0, L_WQ = 16384, L_WKV = 118784, L_PP = 0, L_WO = 0, L_RING = 102400, L_WRED = 102400, L_X = 0, L_XE = 81920, L_STP = 126976, L_PART = 0,
              L_G1 = 147456, L_SA = 150016, L_AUX = 152576, L_MRED = 156416, L_A = 156928, L_OWN = 157184, L_DUMMY = 159744, L_TOTAL = 160768;
constexpr int HS = 1296;
constexpr int NPRE = 26;
constexpr u32 SPIN_CAP = 400000, ONE2 = 0x3F803F80u;
template <int N> struct IC { static constexpr int value = N; };

DI float bflo(u32 v) { return __uint_as_float(v << 16); }
DI float bfhi(u32 v) { return __uint_as_float(v & 0xffff0000u); }
DI u32 pk2(float a, float b) { f32x2 v = {a, b}; return __builtin_bit_cast(u32, __builtin_convertvector(v, bf16x2)); }
DI float bfr(float a) { return bflo(pk2(a, a)); }
DI f32x16 mfma32(v4u a, v4u b, f32x16 c) { return __builtin_amdgcn_mfma_f32_32x32x16_bf16(__builtin_bit_cast(bf16x8, a), __builtin_bit_cast(bf16x8, b), c, 0, 0, 0); }
DI f32x4 mfma16(v4u a, v4u b, f32x4 c) { return __builtin_amdgcn_mfma_f32_16x16x32_bf16(__builtin_bit_cast(bf16x8, a), __builtin_bit_cast(bf16x8, b), c, 0, 0, 0); }
DI void bar() { asm volatile("s_waitcnt lgkmcnt(0)" ::: "memory"); __builtin_amdgcn_s_barrier(); asm volatile("" ::: "memory"); }
DI void vm0() { asm volatile("s_waitcnt vmcnt(0)" ::: "memory"); }
template <int CTRL> DI u32 dppu(u32 v) { return (u32)__builtin_amdgcn_update_dpp(0, (int)v, CTRL, 0xf, 0xf, true); }
template <int CTRL> DI float dpp(float v) { return __uint_as_float(dppu<CTRL>(__float_as_uint(v))); }
constexpr int X1 = 0xB1, X2 = 0x4E, HM = 0x141, R4 = 0x124, R8 = 0x128;
DI float t16(float a, float b) { auto r = __builtin_amdgcn_permlane16_swap(__float_as_uint(a), __float_as_uint(b), false, false); return __uint_as_float(r[0]) + __uint_as_float(r[1]); }
DI float t32(float a, float b) { auto r = __builtin_amdgcn_permlane32_swap(__float_as_uint(a), __float_as_uint(b), false, false); return __uint_as_float(r[0]) + __uint_as_float(r[1]); }
DI float hi32(float a) { auto r = __builtin_amdgcn_permlane32_swap(__float_as_uint(a), __float_as_uint(a), false, false); return __uint_as_float(r[1]); }
DI float sum16(float v) { v += dpp<X1>(v); v += dpp<X2>(v); v += dpp<R4>(v); v += dpp<R8>(v); return v; }
DI u32 umax16(u32 v) { v = max(v, dppu<X1>(v)); v = max(v, dppu<X2>(v)); v = max(v, dppu<R4>(v)); v = max(v, dppu<R8>(v)); return v; }
DI u32 umax64(u32 v) { v = umax16(v); auto r = __builtin_amdgcn_permlane16_swap(v, v, false, false); v = max(r[0], r[1]); auto q = __builtin_amdgcn_permlane32_swap(v, v, false, false); return max(q[0], q[1]); }

DI float dot2(u32 a, u32 b, float c) { return __builtin_amdgcn_fdot2_f32_bf16(__builtin_bit_cast(bf16x2, a), __builtin_bit_cast(bf16x2, b), c, false); }
DI u32 rfl(u32 v) { return (u32)__builtin_amdgcn_readfirstlane((int)v); }
DI float rdl(float v, int l) { return __uint_as_float((u32)__builtin_amdgcn_readlane((int)__float_as_uint(v), l)); }
DI float ln_s(float var) { return bfr(sqrtf(bfr(bfr(var) + 1e-5f))); }

struct Args { const char *x, *wq, *wk, *wv, *wo, *w1, *b1, *w2, *b2, *c, *wmod; char* out; char* ws; };

__global__ __launch_bounds__(512, 2) void fused_block(Args a) {
    extern __shared__ __attribute__((aligned(16))) char lds_[];
    LAS char* lds = (LAS char*)lds_;
    const u32 ldsb = (u32)(size_t)lds;
    const int t = threadIdx.x, lane = t & 63, w = __builtin_amdgcn_readfirstlane(t >> 6);
    const int b = blockIdx.x, xcd = b & 7, j = (b >> 3) & 7, grp = xcd * 4 + (b >> 6), row0 = grp * 64, mi = b >> 3;
    const int hi = lane >> 5, g = lane >> 4, l15 = lane & 15, l31 = lane & 31, q4 = (lane & 15) >> 2, p4 = lane & 3;
    GAS char* ws = (GAS char*)a.ws;
    const __amdgpu_buffer_rsrc_t rs = __builtin_amdgcn_make_buffer_rsrc(a.ws, 0, WS_END, 0x00020000);
#define LD16(off) (*(const LAS v4u*)(lds + (off)))
#define LD8(off) (*(const LAS v2u*)(lds + (off)))
#define LDF4(off) (*(const LAS f32x4*)(lds + (off)))
#define ST16(off, v) (*(LAS v4u*)(lds + (off)) = (v))
#define ST8(off, v) (*(LAS v2u*)(lds + (off)) = (v))
#define STF4(off, v) (*(LAS f32x4*)(lds + (off)) = (v))
#define STF(off, v) (*(LAS float*)(lds + (off)) = (v))
#define LDF(off) (*(const LAS float*)(lds + (off)))
#define TR(off) __builtin_bit_cast(v2u, __builtin_amdgcn_ds_read_tr16_b64_v4i16((LAS s16x4*)(lds + (off))))
#define GL8(p) (*(const GAS v2u*)(p))
#define CLD16(off) __builtin_amdgcn_raw_buffer_load_b128(rs, (off), 0, 16)
#define CST16(off, v) __builtin_amdgcn_raw_buffer_store_b128((v), rs, (off), 0, 16)
#define CST8(off, v) __builtin_amdgcn_raw_buffer_store_b64((v), rs, (off), 0, 16)
#define AG16(d, vo, sb, imm) asm volatile("global_load_dwordx4 %0, %1, %2 offset:%3" : "=v"(d) : "v"(vo), "s"(sb), "n"(imm))
#define AG8(d, vo, sb, imm) asm volatile("global_load_dwordx2 %0, %1, %2 offset:%3" : "=v"(d) : "v"(vo), "s"(sb), "n"(imm))
#define AG2(d, vo, sb, imm) asm volatile("global_load_ushort %0, %1, %2 offset:%3" : "=v"(d) : "v"(vo), "s"(sb), "n"(imm))
#define AC16(d, vo, so, imm) asm volatile("buffer_load_dwordx4 %0, %1, %2, %3 offen offset:%4 sc1" : "=v"(d) : "v"(vo), "s"(rs), "s"(so), "n"(imm))
#define AC8(d, vo, so, imm) asm volatile("buffer_load_dwordx2 %0, %1, %2, %3 offen offset:%4 sc1" : "=v"(d) : "v"(vo), "s"(rs), "s"(so), "n"(imm))
#define DMA16(dst, vo, sb) asm volatile("s_mov_b32 m0, %0\n\ts_nop 0\n\tglobal_load_lds_dwordx4 %1, %2" :: "s"(ldsb + (dst)), "v"(vo), "s"(sb) : "m0", "memory")
#define DMA4(dst, vo, sb) asm volatile("s_mov_b32 m0, %0\n\ts_nop 0\n\tglobal_load_lds_dword %1, %2" :: "s"(ldsb + (dst)), "v"(vo), "s"(sb) : "m0", "memory")
#define WAITV(n, ...) asm volatile("s_waitcnt vmcnt(%c[cnt])" : __VA_ARGS__ : [cnt] "n"(n) : "memory")
#define TIE(...) asm volatile("" : __VA_ARGS__)
#define TS(i)
#define TC(i)
    auto tr2 = [&](u32 o0, u32 o1) { v2u x0 = TR(o0), x1 = TR(o1); return (v4u){x0.x, x0.y, x1.x, x1.y}; };
    TS(0); TC(30);
    if (mi >= 28) {
        u64 pc; u32 len; asm volatile("s_getpc_b64 %0\n.Lfb_code_a:\n\ts_mov_b32 %1, .Lfb_code_b-.Lfb_code_a" : "=s"(pc), "=s"(len));
        const u32 vo = min((u32)((mi - 28) * 512 + t) * 64u, len & ~3u);
        DMA4(L_DUMMY, vo, pc);
    }
    u32 Ev; { const u32 z = 0; asm volatile("global_load_dword %0, %1, %2 sc1" : "=v"(Ev) : "v"(z), "s"(a.ws)); }
    const bool crit = mi < 10;
    const int mcol = crit ? xcd * 160 + mi * 16 : 1280 + xcd * 320 + (min(mi, 29) - 10) * 16;
    v4u wm[3]; u32 cc[3];
    auto mod_load = [&](int col) {
        const u32 mr = t >> 1, r2 = min(mr + 512u, 639u), co = col * 2 + (t & 1) * 16;
        AG16(wm[0], mr * 7680 + co, a.wmod, 0); AG16(wm[1], (mr + 256) * 7680 + co, a.wmod, 0); AG16(wm[2], r2 * 7680 + co, a.wmod, 0);
        AG2(cc[0], mr * 2, a.c, 0); AG2(cc[1], mr * 2, a.c, 512); AG2(cc[2], r2 * 2, a.c, 0);
    };
    auto mod_partial = [&]() {
        float acc[8] = {};
#pragma unroll
        for (int i = 0; i < 3; ++i) {
            const float cv = bflo(cc[i]); float s = bfr(cv * __builtin_amdgcn_rcpf(1.f + __builtin_amdgcn_exp2f(cv * -1.4426950408889634f)));
            if (i == 2 && t >= 256) s = 0.f;
#pragma unroll
            for (int e = 0; e < 4; ++e) { acc[2 * e] += s * bflo(wm[i][e]); acc[2 * e + 1] += s * bfhi(wm[i][e]); }
        }
#pragma unroll
        for (int i = 0; i < 4; ++i) acc[i] = t32(acc[i], acc[i + 4]);
#pragma unroll
        for (int i = 0; i < 2; ++i) { acc[i] = t16(acc[i], acc[i + 2]); acc[i] += dpp<X2>(acc[i]); acc[i] += dpp<R4>(acc[i]); acc[i] += dpp<R8>(acc[i]); }
        if ((lane & 14) == 0) *(LAS f32x2*)(lds + L_MRED + (w * 16 + 8 * (lane & 1) + 4 * hi + 2 * (g & 1)) * 4) = (f32x2){acc[0], acc[1]};
    };
    if (crit) mod_load(mcol);
    const int tq = j >> 1, isV = j & 1;
    v2u xp[5]; v4u xr[10];
    { const u32 vo = (row0 + 16 * tq + 2 * w + hi) * 1280 + l31 * 8;
#pragma unroll
      for (int i = 0; i < 5; ++i) AG8(xp[i], vo, a.x, i * 256); }
    { const char* kp = isV ? a.wv : a.wk;
#pragma unroll
      for (int i = 0; i < 4; ++i) { const u32 o = min(w + 8 * i, 24) * 1024; DMA16(L_WKV + o, o + lane * 16, kp); } }
    { const u32 vo = (row0 + 8 * w) * 1280 + lane * 16, vt = (row0 + 8 * w + 2 * g) * 1280 + 1024 + l15 * 16;
#pragma unroll
      for (int i = 0; i < 8; ++i) AG16(xr[i], vo + (i / 3) * 3840, a.x, (i % 3) * 1280);
      AG16(xr[8], vt, a.x, 0); AG16(xr[9], vt, a.x, 1280); }
    const u32 slice_vo = ((u32)lane / 10u) * 1120 + lane * 16 + j * 160;
    auto slice_dma = [&](const char* W, u32 dst, int k) { const int i = min(w + 8 * k, 106);
        if (k < 13 || i < 106 || lane < 40) DMA16(dst + i * 960, slice_vo + i * 7680, W); };
#pragma unroll
    for (int k = 0; k < 14; ++k) slice_dma(a.wq, L_WQ, k);
    WAITV(33, "+v"(Ev));
    const u32 tag = rfl(Ev) * 1664525u + 1013904223u;
    auto mod_publish = [&](int col) {
        if (t < 8) { float s0 = 0, s1 = 0;
#pragma unroll
            for (int i = 0; i < 8; ++i) { const f32x2 v = *(const LAS f32x2*)(lds + L_MRED + (i * 16 + 2 * t) * 4); s0 += v.x; s1 += v.y; }
            CST8(WS_MODG + (col / 2 + t) * 8, ((v2u){pk2(s0, s1), tag})); } };
    if (crit) { TIE("+v"(wm[0]), "+v"(wm[1]), "+v"(wm[2]), "+v"(cc[0]), "+v"(cc[1]), "+v"(cc[2])); mod_partial(); bar(); mod_publish(mcol); }
    TS(1);
    float xpn[20];
    WAITV(28, "+v"(xp[0]), "+v"(xp[1]), "+v"(xp[2]), "+v"(xp[3]), "+v"(xp[4]));
    {
        float s = 0, q = 0;
#pragma unroll
        for (int i = 0; i < 5; ++i) { s = dot2(xp[i].x, ONE2, s); q = dot2(xp[i].x, xp[i].x, q); s = dot2(xp[i].y, ONE2, s); q = dot2(xp[i].y, xp[i].y, q); }
        s = sum16(s); q = sum16(q); s = t16(s, s); q = t16(q, q);
        const float m = s * (1.f / 640.f), rsd = 1.f / ln_s(fmaxf(q * (1.f / 640.f) - m * m, 0.f)), nb = -bfr(m) * rsd;
#pragma unroll
        for (int i = 0; i < 5; ++i) { xpn[4 * i] = bflo(xp[i].x) * rsd + nb; xpn[4 * i + 1] = bfhi(xp[i].x) * rsd + nb; xpn[4 * i + 2] = bflo(xp[i].y) * rsd + nb; xpn[4 * i + 3] = bfhi(xp[i].y) * rsd + nb; }
    }
    float xn[80];
    WAITV(14, "+v"(xr[0]), "+v"(xr[1]), "+v"(xr[2]), "+v"(xr[3]), "+v"(xr[4]), "+v"(xr[5]), "+v"(xr[6]), "+v"(xr[7]), "+v"(xr[8]), "+v"(xr[9]));
    {
        float s[10], q[10];
#pragma unroll
        for (int i = 0; i < 10; ++i) { s[i] = 0; q[i] = 0;
#pragma unroll
            for (int e = 0; e < 4; ++e) { s[i] = dot2(xr[i][e], ONE2, s[i]); q[i] = dot2(xr[i][e], xr[i][e], q[i]); } }
#pragma unroll
        for (int i = 0; i < 4; ++i) { s[i] = t32(s[i], s[i + 4]); q[i] = t32(q[i], q[i + 4]); }
        float rsd[2], nb[2];
#pragma unroll
        for (int i = 0; i < 2; ++i) { const float ss = sum16(t16(s[i], s[i + 2]) + s[8 + i]), qq = sum16(t16(q[i], q[i + 2]) + q[8 + i]);
            const float m = ss * (1.f / 640.f); rsd[i] = 1.f / ln_s(fmaxf(qq * (1.f / 640.f) - m * m, 0.f)); nb[i] = -bfr(m) * rsd[i]; }
#pragma unroll
        for (int i = 0; i < 10; ++i) { const float r_ = i < 8 ? rdl(rsd[i & 1], 16 * (i >> 1)) : rsd[i & 1], n_ = i < 8 ? rdl(nb[i & 1], 16 * (i >> 1)) : nb[i & 1];
#pragma unroll
            for (int e = 0; e < 4; ++e) { xn[8 * i + 2 * e] = bflo(xr[i][e]) * r_ + n_; xn[8 * i + 2 * e + 1] = bfhi(xr[i][e]) * r_ + n_; }
            TIE("+v"(xn[8 * i]), "+v"(xn[8 * i + 1]), "+v"(xn[8 * i + 2]), "+v"(xn[8 * i + 3]), "+v"(xn[8 * i + 4]), "+v"(xn[8 * i + 5]), "+v"(xn[8 * i + 6]), "+v"(xn[8 * i + 7])); }
    }
    TS(2);
    vm0(); bar();
    TS(3);
    const int hh = w & 3, th = w >> 2;
    v4u wf[NPRE];
    const u32 wqb = L_WQ + (8 * hi + q4) * 160 + (20 * hh + 16 * (g & 1) + 4 * p4) * 2;
#pragma unroll
    for (int ks = 0; ks < NPRE; ++ks) wf[ks] = tr2(wqb + ks * 2560, wqb + ks * 2560 + 640);
    auto gather4 = [&](int col, bool act) {
        v4u v;
        for (u32 sp = 0; sp < SPIN_CAP; ++sp) { v = CLD16(WS_MODG + col * 4); if (__all(!act || (v.y == tag && v.w == tag))) break; __builtin_amdgcn_s_sleep(1); }
        return (v2u){v.x, v.z};
    };
    if (w < 5) { const v2u v = gather4(4 * t, true); f32x4 f = {bflo(v.x), bfhi(v.x), bflo(v.y), bfhi(v.y)};
        if (t >= 160) { f.x = bfr(1.f + f.x); f.y = bfr(1.f + f.y); f.z = bfr(1.f + f.z); f.w = bfr(1.f + f.w); STF4(L_G1 + (t - 160) * 16, f); } else STF4(L_SA + t * 16, f); }
    bar();
    TS(4);
    {
        const u32 hb = L_H + (16 * tq + 2 * w + hi) * HS + l31 * 8;
#pragma unroll
        for (int i = 0; i < 5; ++i) { const f32x4 g0 = LDF4(L_G1 + l31 * 16 + i * 512), s0 = LDF4(L_SA + l31 * 16 + i * 512);
            ST8(hb + i * 256, ((v2u){pk2(xpn[4 * i] * g0.x + s0.x, xpn[4 * i + 1] * g0.y + s0.y), pk2(xpn[4 * i + 2] * g0.z + s0.z, xpn[4 * i + 3] * g0.w + s0.w)})); }
    }
    bar();
    const int fw = (2 * tq + 2) & 7; const u32 kvred = L_H + 8 * fw * HS;
    {
        const int nt = w & 1, kq = w >> 1; f32x4 acc = {};
#pragma unroll
        for (int i = 0; i < 5; ++i) { const int ks = 5 * kq + i;
            const v4u hf = LD16(L_H + (16 * tq + l15) * HS + ks * 64 + g * 16);
            const u32 wb = L_WKV + (32 * ks + 8 * g + q4) * 40 + nt * 32 + p4 * 8; const v4u kf = tr2(wb, wb + 160);
            acc = mfma16(isV ? hf : kf, isV ? kf : hf, acc); }
        STF4(kvred + w * 1024 + lane * 16, acc);
    }
    bar();
    TS(5);
    if (w == fw) {
        f32x4 s0 = {}, s1 = {};
#pragma unroll
        for (int kq = 0; kq < 4; ++kq) { s0 += LDF4(kvred + (2 * kq) * 1024 + lane * 16); s1 += LDF4(kvred + (2 * kq + 1) * 1024 + lane * 16); }
        const v2u p0 = {pk2(s0.x, s0.y), pk2(s0.z, s0.w)}; v2u p1 = {pk2(s1.x, s1.y), pk2(s1.z, s1.w)};
        const int key0 = row0 + 16 * tq; const u32 cb = WS_KV + (key0 >> 7) * KVC, tile = (key0 >> 5) & 3; float n2 = 0;
        if (isV) {
            const u32 o = cb + 4096 + tile * 1536 + (tq & 1) * 768 + (g & 1) * 384 + l15 * 16 + 8 * (g >> 1);
            CST8(o, p0);
            if (l15 >= 4) { const u32 c = l15 == 4 ? ONE2 : 0u; p1 = (v2u){c, c}; }
            if (l15 < 8) CST8(o + 256, p1);
        } else {
            const int kk = (key0 & 31) + l15;
            CST8(cb + tile * 1024 + (kk + 32 * (g & 1)) * 16 + 8 * (g >> 1), p0);
            if (g == 0) CST16(cb + 10240 + tile * 512 + kk * 16, ((v4u){p1.x, p1.y, ONE2, ONE2}));
            n2 = dot2(p0.x, p0.x, dot2(p0.y, p0.y, 0.f));
            if (g == 0) n2 = dot2(p1.x, p1.x, dot2(p1.y, p1.y, n2));
            n2 = t16(n2, n2); n2 = t32(n2, n2);
            n2 = __uint_as_float(umax16(__float_as_uint(n2)));
        }
        vm0();
        if (lane == 0) CST8(WS_KVF + b * 8, ((v2u){__float_as_uint(n2), tag}));
    }
    TS(6);
    {
        const u32 hb = L_H + 8 * w * HS + lane * 16, ht = L_H + (8 * w + 2 * g) * HS + 1024 + l15 * 16;
        f32x4 gm[2][2], sm[2][2];
#pragma unroll
        for (int k = 0; k < 2; ++k) { gm[0][k] = LDF4(L_G1 + lane * 32 + k * 16); sm[0][k] = LDF4(L_SA + lane * 32 + k * 16); gm[1][k] = LDF4(L_G1 + 2048 + l15 * 32 + k * 16); sm[1][k] = LDF4(L_SA + 2048 + l15 * 32 + k * 16); }
#pragma unroll
        for (int i = 0; i < 10; ++i) { const int p = i >= 8; v4u o;
#pragma unroll
            for (int e = 0; e < 4; ++e) o[e] = pk2(xn[8 * i + 2 * e] * gm[p][e >> 1][2 * (e & 1)] + sm[p][e >> 1][2 * (e & 1)], xn[8 * i + 2 * e + 1] * gm[p][e >> 1][2 * (e & 1) + 1] + sm[p][e >> 1][2 * (e & 1) + 1]);
            ST16(p ? ht + (i - 8) * HS : hb + i * HS, o); }
    }
    bar();
    TS(7);
    v4u qf0, qf1; float qn2;
    {
        f32x16 acc = {};
        const u32 hb = L_H + (32 * th + l31) * HS + hi * 16; u32 wq2 = wqb + NPRE * 2560; TIE("+v"(wq2));
#pragma unroll
        for (int ks = 0; ks < 40; ++ks) acc = mfma32(ks < NPRE ? wf[ks < NPRE ? ks : 0] : tr2(wq2 + (ks - NPRE) * 2560, wq2 + (ks - NPRE) * 2560 + 640), LD16(hb + ks * 32), acc);
        const float sc = 0.22360679774997896f * 1.4426950408889634f;
        qf0 = (v4u){pk2(acc[0] * sc, acc[1] * sc), pk2(acc[2] * sc, acc[3] * sc), pk2(acc[4] * sc, acc[5] * sc), pk2(acc[6] * sc, acc[7] * sc)};
        qf1 = (v4u){hi ? 0u : pk2(acc[8] * sc, acc[9] * sc), hi ? 0u : pk2(acc[10] * sc, acc[11] * sc), 0u, 0u};
        qn2 = dot2(qf0.x, qf0.x, dot2(qf0.y, qf0.y, dot2(qf0.z, qf0.z, dot2(qf0.w, qf0.w, dot2(qf1.x, qf1.x, dot2(qf1.y, qf1.y, 0.f))))));
        qn2 = t32(qn2, qn2);
    }
    TS(8);
    float kmax2;
    {
        v4u f0, f1;
        for (u32 sp = 0; sp < SPIN_CAP; ++sp) { f0 = CLD16(WS_KVF + lane * 16); f1 = CLD16(WS_KVF + 1024 + lane * 16);
            if (__all(f0.y == tag && f0.w == tag && f1.y == tag && f1.w == tag)) break; __builtin_amdgcn_s_sleep(1); }
        kmax2 = __uint_as_float(umax64(max(max(f0.x, f0.z), max(f1.x, f1.z))));
    }
    TS(9);
    v4u st0; v2u st1;
    const u32 kvo0 = t * 16, kvo1 = 8192 + t * 8;
    auto kv_load = [&](int c) { AC16(st0, kvo0, WS_KV + c * KVC, 0); AC8(st1, kvo1, WS_KV + c * KVC, 0); };
    auto kv_write = [&](u32 slot) { ST16(slot + kvo0, st0); ST8(slot + kvo1, st1); };
    {
        v4u a0, b0; v2u a1, b1;
        AC16(a0, kvo0, WS_KV, 0); AC8(a1, kvo1, WS_KV, 0); AC16(b0, kvo0, WS_KV + KVC, 0); AC8(b1, kvo1, WS_KV + KVC, 0);
        kv_load(2);
        bar();
        WAITV(4, "+v"(a0), "+v"(a1)); ST16(L_RING + kvo0, a0); ST8(L_RING + kvo1, a1);
        WAITV(2, "+v"(b0), "+v"(b1)); ST16(L_RING + KVC + kvo0, b0); ST8(L_RING + KVC + kvo1, b1);
    }
    if (b == 0 && t == 0) __hip_atomic_store((GAS u32*)(ws + WS_E), tag, RLX_AGENT);
    const float Msh = bfr(sqrtf(qn2 * kmax2) * 1.01f + 1e-30f);
    qf1.z = hi ? 0u : (pk2(-Msh, 0.f) & 0xffffu);
    bar();
    TS(10); TC(28);
    const int ntile = j < 6 ? 7 : 6, nt0 = j < 6 ? 7 * j : 42 + 6 * (j - 6); const bool has_tile = w < ntile;
    const u32 w1o = WS_W1F + (nt0 + min(w, ntile - 1)) * 40960, l16 = lane * 16;
    v4u w1f[40];
    auto pp_item = [&](int r, bool& isw2, int& ks, int& nt) { int i = min(17 * b + min(w + 8 * r, 16), 4319); isw2 = i >= 2160; if (isw2) { i -= 2160; ks = i / 40; nt = i - ks * 40; } else { ks = i / 54; nt = i - ks * 54; } };
    const int mcol2 = 1280 + xcd * 320 + (min(max(mi, 10), 29) - 10) * 16; const bool defer = mi >= 10 && mi < 30;
    f32x16 oacc = {};
    {
        const u32 ak0 = L_RING + lane * 16, ak1 = L_RING + 10240 + l31 * 16, av = L_RING + 4096 + (hi * 24 + (l31 < 24 ? l31 : (l31 < 28 ? l31 - 4 : l31 - 12))) * 16;
        v4u k0 = LD16(ak0), k1 = LD16(ak1);
        f32x16 sc = mfma32(k0, qf0, (f32x16){}); sc = mfma32(k1, qf1, sc);
        k0 = LD16(ak0 + 1024); k1 = LD16(ak1 + 512);
        v4u p1 = {}, v1 = {}, pf0, pf1, mg;
#define EX(i) __builtin_amdgcn_exp2f(sc[i])
        auto chunk = [&](auto CC) {
            constexpr int c = decltype(CC)::value; constexpr u32 cur = (c % 3) * KVC, nxt = ((c + 1) % 3) * KVC, fre = ((c + 2) % 3) * KVC;
            constexpr int pc = c - 1, NY = pc == 0 ? 9 : (pc == 3 || pc == 4) ? 4 : (pc == 5 || pc == 6) ? 3 : pc == 8 ? 6 : pc > 8 ? 4 : 0;
#pragma unroll
            for (int i = 0; i < 4; ++i) {
                v4u p0; f32x16 sn;
                const v4u v0 = LD16(cur + av + i * 1536);
                const u32 kb = i < 2 ? cur + (i + 2) * 1024 : nxt + (i - 2) * 1024, kb1 = i < 2 ? cur + (i + 2) * 512 : nxt + (i - 2) * 512;
                SB();
                sn = mfma32(k0, qf0, (f32x16){});
                k0 = LD16(kb + ak0);
                p0.x = pk2(EX(0), EX(1)); p0.y = pk2(EX(2), EX(3));
                SB();
                oacc = mfma32(v1, p1, oacc);
                v1 = LD16(cur + av + i * 1536 + 768);
                p0.z = pk2(EX(4), EX(5)); p0.w = pk2(EX(6), EX(7));
                SB();
                sn = mfma32(k1, qf1, sn);
                k1 = LD16(kb1 + ak1);
                p1.x = pk2(EX(8), EX(9)); p1.y = pk2(EX(10), EX(11));
                SB();
                oacc = mfma32(v0, p0, oacc);
                p1.z = pk2(EX(12), EX(13)); p1.w = pk2(EX(14), EX(15));
                TIE("+v"(p1));
                if (i == 0) {
                    if constexpr (c < 14) { WAITV(NY, "+v"(st0), "+v"(st1)); kv_write(L_RING + fre); }
                    if constexpr (c == 2) {
#pragma unroll
                        for (int r = 0; r < 3; ++r) { bool isw2; int ks, nt; pp_item(r, isw2, ks, nt);
                            const u32 o = L_PP + (w + 8 * r) * 1024 + (isw2 ? (16 * (g >> 1) + 4 * (g & 1) + q4) * 32 + p4 * 8 : (4 * hi + q4) * 64 + (g & 1) * 32 + p4 * 8);
                            const v4u f = tr2(o, o + (isw2 ? 256 : 512));
                            CST16((isw2 ? WS_W2F + (nt * 54 + ks) * 1024 : WS_W1F + (nt * 40 + ks) * 1024) + l16, f); }
                        TIE("+v"(wm[0]), "+v"(wm[1]), "+v"(wm[2]), "+v"(cc[0]), "+v"(cc[1]), "+v"(cc[2]));
                        if (defer) mod_partial();
                    }
                    if constexpr (c == 3) { if (defer) mod_publish(mcol2); }
                    if constexpr (c == 4) { if (t == 0) CST8(WS_WF + b * 8, ((v2u){0u, tag})); }
                    if constexpr (c == 8) {
                        TIE("+v"(pf0), "+v"(pf1), "+v"(mg));
                        for (u32 sp = 0; sp < SPIN_CAP; ++sp) { if (__all(pf0.y == tag && pf0.w == tag && pf1.y == tag && pf1.w == tag)) break;
                            __builtin_amdgcn_s_sleep(1); pf0 = CLD16(WS_WF + l16); pf1 = CLD16(WS_WF + 1024 + l16); }
                        if (w < 6) {
                            const bool act = t < 360; const int u = t - 320;
                            v2u v = {mg.x, mg.z};
                            if (!__all(!act || (mg.y == tag && mg.w == tag))) v = gather4(t < 320 ? 1920 + 4 * t : (u < 20 ? 1280 + 80 * j + 4 * u : 3200 + 80 * j + 4 * (min(u, 39) - 20)), act);
                            f32x4 f = {bflo(v.x), bfhi(v.x), bflo(v.y), bfhi(v.y)};
                            if (t < 320) {
                                const int isg = t >= 160, cl = 4 * (t - 160 * isg), jj = cl / 80, r = cl - jj * 80;
                                if (isg) { v = (v2u){pk2(1.f + f.x, 1.f + f.y), pk2(1.f + f.z, 1.f + f.w)}; f = (f32x4){bflo(v.x), bfhi(v.x), bflo(v.y), bfhi(v.y)}; }
                                const int gg = (r >> 2) & 3, ks = r < 64 ? 5 * jj + 2 * (r >> 5) + (gg >> 1) : 5 * jj + 4, h2 = gg & 1, tt = r < 64 ? (r >> 4) & 1 : (r >> 3) & 1;
                                ST8(L_AUX + (isg ? 0 : 1280) + (ks * 2 + h2) * 16 + tt * 8, v);
                                if (jj == j) { if (isg) STF4(L_OWN + 320 + r * 4, f); ST8(L_OWN + (isg ? 960 : 1120) + r * 2, v); }
                            } else if (act) STF4(L_OWN + (u < 20 ? u * 16 : 640 + (u - 20) * 16), f);
                        } else if (t < 464) ST16(L_AUX + 2560 + (t - 384) * 16, ((v4u){ONE2, ONE2, ONE2, ONE2}));
                    }
                }
                if (i == 1) {
                    if constexpr (c == 7) { const int u = t - 320; const u32 mo = (t < 320 ? 1920 + 4 * t : (u < 20 ? 1280 + 80 * j + 4 * u : 3200 + 80 * j + 4 * (min(u, 39) - 20))) * 4;
                        AC16(pf0, l16, WS_WF, 0); AC16(pf1, l16, WS_WF, 1024); AC16(mg, mo, WS_MODG, 0); }
                    if constexpr (c < 13) kv_load(c + 3);
                    if constexpr (c == 0) {
#pragma unroll
                        for (int r = 0; r < 3; ++r) { bool isw2; int ks, nt; pp_item(r, isw2, ks, nt); u32 vo;
                            if (isw2) vo = (32 * ks + (lane >> 1)) * 1280 + nt * 32 + (lane & 1) * 16;
                            else { const int jj = ks / 5, ii = ks - jj * 5, lr = lane >> 2; vo = (ii < 4 ? 80 * jj + 32 * (ii >> 1) + 8 * (ii & 1) + 16 * (lr >> 3) + (lr & 7) : 80 * jj + 64 + lr) * 3456 + nt * 64 + p4 * 16; }
                            DMA16(L_PP + (w + 8 * r) * 1024, vo, isw2 ? a.w2 : a.w1); }
                        mod_load(mcol2);
                    }
                    if constexpr (c >= 3 && c <= 6) {
#pragma unroll
                        for (int k = (c < 5 ? 4 * (c - 3) : 8 + 3 * (c - 5)); k < (c < 5 ? 4 * (c - 2) : 8 + 3 * (c - 4)); ++k) slice_dma(a.wo, L_WO, k); }
                    if constexpr (c == 8) {
#pragma unroll
                        for (int r = 0; r < 2; ++r) DMA4(L_DUMMY, WS_W2F + min((u32)(mi * 512 + t + r * 16384), 17279u) * 128u, a.ws); }
                    if constexpr (c >= 8) {
#pragma unroll
                        for (int k = 4 * (c - 8); k < 4 * (c - 7); ++k) AC16(w1f[k], l16, w1o + (k & ~3) * 1024, (k & 3) * 1024); }
                }
                SB();
                sc = sn;
            }
            bar();
        };
        chunk(IC<0>{}); chunk(IC<1>{}); chunk(IC<2>{}); chunk(IC<3>{}); chunk(IC<4>{}); chunk(IC<5>{}); chunk(IC<6>{}); chunk(IC<7>{});
        chunk(IC<8>{}); chunk(IC<9>{}); chunk(IC<10>{}); chunk(IC<11>{}); chunk(IC<12>{}); chunk(IC<13>{}); chunk(IC<14>{}); chunk(IC<15>{});
        oacc = mfma32(v1, p1, oacc);
    }
    TS(11); TC(29);
    float lsum = hi32(oacc[8]);
    if (__any(!(lsum >= 1e-12f && lsum < 3e38f))) {
        qf1.z = 0u;
        const u32 ak0 = WS_KV + lane * 16, ak1 = WS_KV + 10240 + l31 * 16, av = WS_KV + 4096 + (hi * 24 + (l31 < 24 ? l31 : 20)) * 16;
        float mx = -__builtin_inff();
        for (int tI = 0; tI < 64; ++tI) { const u32 cb = (tI >> 2) * KVC, i = tI & 3;
            const v4u k0 = CLD16(cb + ak0 + i * 1024), k1 = CLD16(cb + ak1 + i * 512);
            f32x16 s = mfma32(k0, qf0, (f32x16){}); s = mfma32(k1, qf1, s);
#pragma unroll
            for (int r = 0; r < 16; ++r) mx = fmaxf(mx, s[r]); }
        mx = fmaxf(mx, __shfl_xor(mx, 32));
        oacc = (f32x16){};
        for (int tI = 0; tI < 64; ++tI) { const u32 cb = (tI >> 2) * KVC, i = tI & 3;
            const v4u k0 = CLD16(cb + ak0 + i * 1024), k1 = CLD16(cb + ak1 + i * 512);
            const v4u v0 = CLD16(cb + av + i * 1536), v1 = CLD16(cb + av + i * 1536 + 768);
            f32x16 s = mfma32(k0, qf0, (f32x16){}); s = mfma32(k1, qf1, s);
            v4u p0, p1;
#pragma unroll
            for (int e = 0; e < 4; ++e) { p0[e] = pk2(__builtin_amdgcn_exp2f(s[2 * e] - mx), __builtin_amdgcn_exp2f(s[2 * e + 1] - mx));
                                          p1[e] = pk2(__builtin_amdgcn_exp2f(s[8 + 2 * e] - mx), __builtin_amdgcn_exp2f(s[9 + 2 * e] - mx)); }
            oacc = mfma32(v0, p0, oacc); oacc = mfma32(v1, p1, oacc); }
        lsum = hi32(oacc[8]);
    }
    const u32 gfl = WS_GF + grp * 1024;
    const int mp = w >> 1, par = w & 1;
    v2u xres[3]; u32 b1v;
    {
        const float il = __builtin_amdgcn_rcpf(lsum); const int mt = 2 * th + (l31 >> 4);
        const v4u o0 = {pk2(oacc[0] * il, oacc[1] * il), pk2(oacc[2] * il, oacc[3] * il), pk2(oacc[4] * il, oacc[5] * il), pk2(oacc[6] * il, oacc[7] * il)};
        const u32 ob = WS_O + grp * 81920 + mt * 20480 + l15 * 16;
        CST16(ob + (2 * j + (hh >> 1)) * 1024 + (2 * (hh & 1) + hi) * 256, o0);
        const int ci = 64 + 2 * j + (hh >> 1);
        if (!hi) CST8(ob + ci * 256 + 8 * (hh & 1), ((v2u){pk2(oacc[8] * il, oacc[9] * il), pk2(oacc[10] * il, oacc[11] * il)}));
        SB();
#pragma unroll
        for (int k = 32; k < 40; ++k) AC16(w1f[k], l16, w1o + (k & ~3) * 1024, (k & 3) * 1024);
        const u32 xo = (row0 + 16 * mp + l15) * 1280 + j * 160 + g * 8;
        AG8(xres[0], xo + par * 64, a.x, 0); AG8(xres[1], xo + par * 64, a.x, 32); AG8(xres[2], xo, a.x, 128);
        AG2(b1v, ((nt0 + min(w, ntile - 1)) * 32 + l31) * 2, a.b1, 0);
    }
#define W1T(k) "+v"(w1f[k]), "+v"(w1f[k + 1]), "+v"(w1f[k + 2]), "+v"(w1f[k + 3]), "+v"(w1f[k + 4]), "+v"(w1f[k + 5]), "+v"(w1f[k + 6]), "+v"(w1f[k + 7])
    WAITV(12, W1T(0), W1T(8)); TIE(W1T(16), W1T(24));
    bar();
    if (t == 0) __hip_atomic_store((GAS u32*)(ws + gfl + j * 128), tag, RLX_AGENT);
    TS(12);
    auto wait_flags = [&](int phase, int first, int n) {
        for (u32 sp = 0; sp < SPIN_CAP; ++sp) { const u32 f = __builtin_amdgcn_raw_buffer_load_b32(rs, gfl + phase * 32768 + (first + (lane & (n - 1))) * 128, 0, 16);
            if (__all(f == tag)) break; __builtin_amdgcn_s_sleep(1); } };
    f32x16 ua = {};
    const u32 ab = L_AUX + min(l31, 2) * 1280 + hi * 16;
#pragma unroll
    for (int ks = 0; ks < 24; ++ks) ua = mfma32(LD16(ab + ks * 32), w1f[ks], ua);
    SB();
    wait_flags(0, 4 * par, 4);
    WAITV(0, W1T(32), "+v"(xres[0]), "+v"(xres[1]), "+v"(xres[2]), "+v"(b1v));
    TS(13);
    u32 x1p[6]; v4u wex;
    {
        v4u xf[10];
        const u32 ob = WS_O + grp * 81920 + mp * 20480 + l16;
#pragma unroll
        for (int i = 0; i < 10; ++i) xf[i] = CLD16(ob + (i < 8 ? 8 * par + i : 16 + 2 * par + (i - 8)) * 1024);
        SB();
#pragma unroll
        for (int ks = 24; ks < 40; ++ks) ua = mfma32(LD16(ab + ks * 32), w1f[ks], ua);
        {
            const u32 h0 = pk2(ua[0], ua[1]), h2 = pk2(ua[2], 0.f);
            const u32 l0 = pk2(ua[0] - bflo(h0), ua[1] - bfhi(h0)), l2 = pk2(ua[2] - bflo(h2), 0.f);
            wex.x = hi ? (b1v | (b1v << 16)) : ((h0 & 0xffffu) | (l0 << 16)); wex.y = hi ? 0u : ((h0 >> 16) | (l0 & 0xffff0000u));
            wex.z = hi ? 0u : ((h2 & 0xffffu) | (l2 << 16)); wex.w = hi ? 0u : (h2 & 0xffffu);
            TIE("+v"(wex));
        }
        SB();
        f32x4 acc[5] = {};
        const u32 wm_ = L_WO + (320 * par + 20 * (g >> 1) + 4 * (g & 1) + q4) * 160 + p4 * 8, wl_ = L_WO + (320 * par + 40 * g + 16 + q4) * 160 + p4 * 8;
#pragma unroll
        for (int i = 0; i < 10; ++i)
#pragma unroll
            for (int nt = 0; nt < 5; ++nt) {
                const u32 o = i < 8 ? wm_ + i * 6400 + nt * 32 : wl_ + (i - 8) * 25600 + nt * 32;
                acc[nt] = mfma16(tr2(o, o + (i < 8 ? 1280 : 3200)), xf[i], acc[nt]); }
        const u32 rb = L_WRED + w * 3072 + l16;
        STF4(rb, par ? acc[0] : acc[2]); STF4(rb + 1024, par ? acc[1] : acc[3]); STF4(rb + 2048, acc[4]);
        bar();
        TS(22);
        const u32 pb = L_WRED + (w ^ 1) * 3072 + l16;
        f32x4 r[3]; r[0] = (par ? acc[2] : acc[0]) + LDF4(pb); r[1] = (par ? acc[3] : acc[1]) + LDF4(pb + 1024); r[2] = acc[4] + LDF4(pb + 2048);
        u32 xg[6]; v2u g2p[3], sbp[3];
#pragma unroll
        for (int i = 0; i < 3; ++i) {
            const u32 co = (i < 2 ? par * 128 + i * 64 : 256) + g * 16;
            const f32x4 ga = LDF4(L_OWN + co), g2 = LDF4(L_OWN + 320 + co); g2p[i] = LD8(L_OWN + 960 + co / 2); sbp[i] = LD8(L_OWN + 1120 + co / 2);
            x1p[2 * i] = pk2(r[i][0] * ga[0] + bflo(xres[i].x), r[i][1] * ga[1] + bfhi(xres[i].x)); x1p[2 * i + 1] = pk2(r[i][2] * ga[2] + bflo(xres[i].y), r[i][3] * ga[3] + bfhi(xres[i].y));
            xg[2 * i] = pk2(bflo(x1p[2 * i]) * g2[0], bfhi(x1p[2 * i]) * g2[1]); xg[2 * i + 1] = pk2(bflo(x1p[2 * i + 1]) * g2[2], bfhi(x1p[2 * i + 1]) * g2[3]);
        }
        const u32 xb = WS_XG + grp * 81920 + (mp >> 1) * 40960 + (16 * (mp & 1) + l15 + 32 * (g & 1)) * 16;
        CST16(xb + (5 * j + 2 * par + (g >> 1)) * 1024, ((v4u){xg[0], xg[1], xg[2], xg[3]}));
        if (hi == par) CST8(xb + (5 * j + 4) * 1024 + 8 * (g >> 1), ((v2u){xg[4], xg[5]}));
        float sm[6] = {};
#pragma unroll
        for (int i = 0; i < 6; ++i) { const bool off = i >= 4 && hi != par; const u32 xa = off ? 0u : x1p[i], ya = off ? 0u : xg[i];
            sm[0] = dot2(xa, ONE2, sm[0]); sm[1] = dot2(xa, xa, sm[1]); sm[2] = dot2(ya, ONE2, sm[2]); sm[3] = dot2(ya, ya, sm[3]);
            sm[4] = dot2(ya, g2p[i >> 1][i & 1], sm[4]); sm[5] = dot2(ya, sbp[i >> 1][i & 1], sm[5]); }
        const float e0 = t32(sm[0], sm[4]), e1 = t32(sm[1], sm[5]), e2 = t32(sm[2], sm[2]), e3 = t32(sm[3], sm[3]);
        const float f0 = t16(e0, e2), f1 = t16(e1, e3);
        if (g < 3) CST8(WS_ST + ((grp * 8 + j) * 64 + 16 * mp + l15) * 64 + par * 32 + g * 8, ((v2u){__float_as_uint(f0), __float_as_uint(f1)}));
    }
    vm0(); bar();
    if (t == 0) __hip_atomic_store((GAS u32*)(ws + gfl + 32768 + j * 128), tag, RLX_AGENT);
    TS(14);
    float cst[5];
    if (w == 7) {
#pragma unroll
        for (int i = 0; i < 5; ++i) cst[i] = 0;
#pragma unroll
        for (int r = 0; r < 2; ++r) { const int ch = lane + 64 * r; v4u gv = LD16(L_AUX + min(ch, 79) * 16), sv = LD16(L_AUX + 1280 + min(ch, 79) * 16);
#pragma unroll
            for (int e = 0; e < 4; ++e) { const u32 g_ = ch < 80 ? gv[e] : 0u, s_ = ch < 80 ? sv[e] : 0u;
                cst[0] = dot2(g_, ONE2, cst[0]); cst[1] = dot2(g_, g_, cst[1]); cst[2] = dot2(g_, s_, cst[2]); cst[3] = dot2(s_, ONE2, cst[3]); cst[4] = dot2(s_, s_, cst[4]); } }
#pragma unroll
        for (int i = 0; i < 5; ++i) { cst[i] = sum16(cst[i]); cst[i] = t16(cst[i], cst[i]); cst[i] = t32(cst[i], cst[i]); }
    }
    wait_flags(1, w, 1);
    TS(15);
    {
        v4u xi[10]; const u32 xb = WS_XG + grp * 81920 + 5 * w * 1024 + l16;
#pragma unroll
        for (int i = 0; i < 10; ++i) xi[i] = CLD16(xb + (i / 5) * 40960 + (i % 5) * 1024);
        const u32 sbo = WS_ST + ((grp * 8 + w) * 64 + lane) * 64;
        const v4u s0 = CLD16(sbo), s1 = CLD16(sbo + 16), s2 = CLD16(sbo + 32), s3 = CLD16(sbo + 48);
#pragma unroll
        for (int i = 0; i < 10; ++i) ST16(L_X + (i / 5) * 40960 + (5 * w + i % 5) * 1024 + l16, xi[i]);
        STF4(L_STP + w * 2048 + l16, __builtin_bit_cast(f32x4, s0) + __builtin_bit_cast(f32x4, s2));
        STF4(L_STP + w * 2048 + 1024 + l16, __builtin_bit_cast(f32x4, s1) + __builtin_bit_cast(f32x4, s3));
    }
    bar();
    TS(16);
    const int ks0 = w < 6 ? 7 * w : 42 + 6 * (w - 6), nks = w < 6 ? 7 : 6;
    v2u b2v[3];
#define AC16R(d, vo, so, imm) asm volatile("buffer_load_dwordx4 %0, %1, %2, %3 offen offset:%4 sc1" : "+v"(d) : "v"(vo), "s"(rs), "s"(so), "n"(imm))
    {
        const u32 w2o = WS_W2F + (5 * j * 54 + ks0) * 1024, w2l = WS_W2F + (5 * j * 54 + ks0 + nks - 1) * 1024;
        auto w2_load = [&](int k) { const int i = k / 5, nt = k % 5; if (i < 6) AC16R(w1f[k], l16, w2o + nt * 55296 + (i & ~3) * 1024, (i & 3) * 1024); else AC16R(w1f[k], l16, w2l + nt * 55296, 0); };
        f32x16 a0 = {}, a1 = {};
        if (w == 7) {
            f32x4 u0 = {}, u1 = {};
#pragma unroll
            for (int i = 0; i < 8; ++i) { u0 += LDF4(L_STP + i * 2048 + l16); u1 += LDF4(L_STP + i * 2048 + 1024 + l16); }
            const float in = 1.f / 640.f, m1 = u0.x * in, m1b = bfr(m1), s1 = ln_s(fmaxf(u0.y * in - m1 * m1, 0.f)), r1 = 1.f / s1;
            const float m2 = (r1 * (u0.z - m1b * cst[0]) + cst[3]) * in;
            const float q2 = (r1 * r1 * (u0.w - 2.f * m1b * u1.x + m1b * m1b * cst[1]) + 2.f * r1 * (u1.y - m1b * cst[2]) + cst[4]) * in;
            const float s2 = ln_s(fmaxf(q2 - m2 * m2, 0.f)), bw = -bfr(m2) * s1, gm = s1 * s2;
            const u32 bh = pk2(bw, gm), bl = pk2(bw - bflo(bh), gm - bfhi(bh));
            const u32 xe = L_XE + hi * 1024 + l31 * 16;
            ST16(xe, ((v4u){pk2(-m1b, -m1b), pk2(s1, s1), (bh & 0xffffu) | (bh << 16), bl & 0xffffu}));
            ST16(xe + 512, ((v4u){(bh >> 16) | (bl & 0xffff0000u), 0u, 0u, 0u}));
            STF(L_A + lane * 4, 1.f / gm);
        }
        {
            u32 xa = L_X + l16, xb2 = L_X + 40960 + l16; TIE("+v"(xb2));
            v4u x0[3], x1[3];
#pragma unroll
            for (int ks = 0; ks < 2; ++ks) { x0[ks] = LD16(xa + ks * 1024); x1[ks] = LD16(xb2 + ks * 1024); }
#pragma unroll
            for (int ks = 0; ks < 40; ++ks) {
                if (ks + 2 < 40) { x0[(ks + 2) % 3] = LD16(xa + (ks + 2) * 1024); x1[(ks + 2) % 3] = LD16(xb2 + (ks + 2) * 1024); }
                SB();
                a0 = mfma32(w1f[ks], x0[ks % 3], a0); a1 = mfma32(w1f[ks], x1[ks % 3], a1);
                SB();
                if (ks >= 1 && ks < 36) w2_load(ks - 1); }
        }
        { const u32 bo = (80 * j + par * 32 + g * 4) * 2; AG8(b2v[0], bo, a.b2, 0); AG8(b2v[1], bo, a.b2, 32); AG8(b2v[2], (80 * j + 64 + g * 4) * 2, a.b2, 0); }
        bar();
        TS(17);
        if (has_tile) {
            a0 = mfma32(wex, LD16(L_XE + l16), a0); a1 = mfma32(wex, LD16(L_XE + 1024 + l16), a1);
            const float A0 = LDF(L_A + l31 * 4), A1 = LDF(L_A + 128 + l31 * 4);
            const u32 hb = WS_H + grp * 221184 + ((l31 >> 4) * 54 + nt0 + w) * 1024 + (l15 + 16 * hi) * 16;
            const float k1 = -2.f * 0.7978845608028654f * 1.4426950408889634f, k3 = k1 * 0.044715f;
            auto gelu = [&](float z) { return z * __builtin_amdgcn_rcpf(1.f + __builtin_amdgcn_exp2f(z * (k1 + k3 * z * z))); };
#pragma unroll
            for (int tt = 0; tt < 2; ++tt)
#pragma unroll
                for (int hf = 0; hf < 2; ++hf) { v4u o; const float A = tt ? A1 : A0;
#pragma unroll
                    for (int e = 0; e < 4; ++e) { const int r = 8 * hf + 2 * e; o[e] = pk2(gelu(A * (tt ? a1[r] : a0[r])), gelu(A * (tt ? a1[r + 1] : a0[r + 1]))); }
                    CST16(hb + tt * 108 * 1024 + hf * 512, o); }
        }
    }
    WAITV(0, W1T(0), W1T(8), "+v"(b2v[0]), "+v"(b2v[1]), "+v"(b2v[2])); TIE(W1T(16), W1T(24), "+v"(w1f[32]), "+v"(w1f[33]), "+v"(w1f[34]));
    bar();
    if (t == 0) __hip_atomic_store((GAS u32*)(ws + gfl + 65536 + j * 128), tag, RLX_AGENT);
    TS(18);
    wait_flags(2, w, 1);
    TS(19);
    {
        f32x4 own[3];
        const u32 hb = WS_H + grp * 221184 + ks0 * 1024 + l16;
#pragma unroll
        for (int half = 0; half < 2; ++half) {
            v4u hf[2][7];
#pragma unroll
            for (int m = 0; m < 2; ++m) { const int mt = (mp + 1 + 2 * half + m) & 3;
#pragma unroll
                for (int i = 0; i < 7; ++i) hf[m][i] = CLD16(hb + (mt * 54 + min(i, nks - 1)) * 1024); }
#pragma unroll
            for (int m = 0; m < 2; ++m) { const int mt = (mp + 1 + 2 * half + m) & 3;
                f32x4 acc[5] = {};
                if (nks < 7) hf[m][6] = (v4u){};
#pragma unroll
                for (int i = 0; i < 7; ++i)
#pragma unroll
                    for (int nt = 0; nt < 5; ++nt) acc[nt] = mfma16(w1f[i * 5 + nt], hf[m][i], acc[nt]);
                if (half == 1 && m == 1) {
                    const u32 pb = L_PART + mt * 35840 + 6 * 5120 + l16;
                    STF4(pb + (par ? 0 : 2048), par ? acc[0] : acc[2]); STF4(pb + (par ? 1024 : 3072), par ? acc[1] : acc[3]);
                    if (hi != par) STF4(pb + 4096, acc[4]);
                    own[0] = par ? acc[2] : acc[0]; own[1] = par ? acc[3] : acc[1]; own[2] = acc[4];
                } else {
                    const u32 pb = L_PART + mt * 35840 + ((w - 2 * mt - 2) & 7) * 5120 + l16;
#pragma unroll
                    for (int nt = 0; nt < 5; ++nt) STF4(pb + nt * 1024, acc[nt]);
                }
            }
        }
        bar();
        TS(20);
        GAS char* op = (GAS char*)a.out + (size_t)(row0 + 16 * mp + l15) * 1280 + j * 160 + g * 8;
#pragma unroll
        for (int i = 0; i < 3; ++i) {
            const u32 pb = L_PART + mp * 35840 + (i < 2 ? 2 * par + i : 4) * 1024 + l16; f32x4 s = own[i];
#pragma unroll
            for (int k = 0; k < 7; ++k) s += LDF4(pb + k * 5120);
            const f32x4 gb = LDF4(L_OWN + 640 + ((i < 2 ? par * 32 + i * 16 : 64) + g * 4) * 4); const v2u bb = b2v[i];
            const float o0 = bflo(x1p[2 * i]) + (s.x + bflo(bb.x)) * gb.x, o1 = bfhi(x1p[2 * i]) + (s.y + bfhi(bb.x)) * gb.y;
            const float o2 = bflo(x1p[2 * i + 1]) + (s.z + bflo(bb.y)) * gb.z, o3 = bfhi(x1p[2 * i + 1]) + (s.w + bfhi(bb.y)) * gb.w;
            if (i < 2 || hi == par) *(GAS v2u*)(op + (i < 2 ? par * 64 + i * 32 : 128)) = (v2u){pk2(o0, o1), pk2(o2, o3)};
        }
    }
    TS(21); TC(31);
    asm volatile(".Lfb_code_b:");
}

extern "C" void kernel_launch(void* const* d_in, const int* in_sizes, int n_in, void* d_out, int out_size, void* d_ws, size_t ws_size, hipStream_t stream) {
    Args a;
    a.x = (const char*)d_in[0]; a.wq = (const char*)d_in[1]; a.wk = (const char*)d_in[2]; a.wv = (const char*)d_in[3]; a.wo = (const char*)d_in[4];
    a.w1 = (const char*)d_in[6]; a.b1 = (const char*)d_in[7]; a.w2 = (const char*)d_in[8]; a.b2 = (const char*)d_in[9]; a.c = (const char*)d_in[10]; a.wmod = (const char*)d_in[11];
    a.out = (char*)d_out; a.ws = (char*)d_ws;
    (void)hipFuncSetAttribute((const void*)fused_block, hipFuncAttributeMaxDynamicSharedMemorySize, L_TOTAL);
    hipLaunchKernelGGL(fused_block, dim3(256), dim3(512), L_TOTAL, stream, a);
}
```

```cpp
#include <hip/hip_runtime.h>
#include <hip/hip_bf16.h>
typedef unsigned u32; typedef unsigned long long u64; typedef unsigned short u16;
typedef u32 v4u __attribute__((ext_vector_type(4)));
typedef u32 v2u __attribute__((ext_vector_type(2)));
typedef float f32x2 __attribute__((ext_vector_type(2)));
typedef float f32x4 __attribute__((ext_vector_type(4)));
typedef float f32x16 __attribute__((ext_vector_type(16)));
typedef __bf16 bf16x8 __attribute__((ext_vector_type(8)));
typedef __bf16 bf16x2 __attribute__((ext_vector_type(2)));
typedef short s16x4 __attribute__((ext_vector_type(4)));
#define GAS __attribute__((address_space(1)))
#define LAS __attribute__((address_space(3)))
#define RLX_AGENT __ATOMIC_RELAXED, __HIP_MEMORY_SCOPE_AGENT
#define DI __device__ __forceinline__
#define SB() __builtin_amdgcn_sched_barrier(0)

constexpr u32 KVC = 12288;
constexpr u32 WS_E = 0, WS_MODG = 1024, WS_KVF = 16384, WS_WF = 18432, WS_GF = 32768, WS_KV = 163840, WS_O = WS_KV + 16 * KVC,
              WS_XG = WS_O + 32 * 81920, WS_ST = WS_XG + 32 * 81920, WS_H = WS_ST + 32 * 8 * 64 * 64, WS_W1F = WS_H + 32 * 221184,
              WS_W2F = WS_W1F + 2160 * 1024, WS_END = WS_W2F + 2160 * 1024, WS_DBG = 20u << 20;
constexpr u32 L_H = 0, L_WQ = 82944, L_WKV = 0, L_XK = 25600, L_PP = 139264, L_WO = 0, L_RING = 102400, L_WRED = 102400, L_X = 0, L_XE = 81920, L_STP = 126976, L_PART = 0,
              L_SA = 152064, L_G1 = 154624, L_AUX = 157184, L_MRED = 161024, L_A = 161536, L_OWN = 161792, L_DUMMY = 163328, L_TOTAL = 163840;
constexpr int HS = 1296;
constexpr int NPRE = 13;
#define QD 6
#define WD 6
#define KVF_I 6
#define POLLW 3
#define PSLEEP 4
#define TOUCH_OWN 1
#define UA1 24
#define WOD 8
#define TOUCH_S 128u
constexpr u32 TOUCH_N = 1280 / TOUCH_S;
constexpr u32 SPIN_CAP = 400000, ONE2 = 0x3F803F80u;
template <int N> struct IC { static constexpr int value = N; };

DI float bflo(u32 v) { return __uint_as_float(v << 16); }
DI float bfhi(u32 v) { return __uint_as_float(v & 0xffff0000u); }
DI u32 pk2(float a, float b) { f32x2 v = {a, b}; return __builtin_bit_cast(u32, __builtin_convertvector(v, bf16x2)); }
DI float bfr(float a) { return bflo(pk2(a, a)); }
DI f32x16 mfma32(v4u a, v4u b, f32x16 c) { return __builtin_amdgcn_mfma_f32_32x32x16_bf16(__builtin_bit_cast(bf16x8, a), __builtin_bit_cast(bf16x8, b), c, 0, 0, 0); }
DI f32x4 mfma16(v4u a, v4u b, f32x4 c) { return __builtin_amdgcn_mfma_f32_16x16x32_bf16(__builtin_bit_cast(bf16x8, a), __builtin_bit_cast(bf16x8, b), c, 0, 0, 0); }
DI void bar() { asm volatile("s_waitcnt lgkmcnt(0)" ::: "memory"); __builtin_amdgcn_s_barrier(); asm volatile("" ::: "memory"); }
DI void vm0() { asm volatile("s_waitcnt vmcnt(0)" ::: "memory"); }
template <int CTRL> DI u32 dppu(u32 v) { return (u32)__builtin_amdgcn_update_dpp(0, (int)v, CTRL, 0xf, 0xf, true); }
template <int CTRL> DI float dpp(float v) { return __uint_as_float(dppu<CTRL>(__float_as_uint(v))); }
constexpr int X1 = 0xB1, X2 = 0x4E, HM = 0x141, R4 = 0x124, R8 = 0x128;
DI float t16(float a, float b) { auto r = __builtin_amdgcn_permlane16_swap(__float_as_uint(a), __float_as_uint(b), false, false); return __uint_as_float(r[0]) + __uint_as_float(r[1]); }
DI float t32(float a, float b) { auto r = __builtin_amdgcn_permlane32_swap(__float_as_uint(a), __float_as_uint(b), false, false); return __uint_as_float(r[0]) + __uint_as_float(r[1]); }
DI float hi32(float a) { auto r = __builtin_amdgcn_permlane32_swap(__float_as_uint(a), __float_as_uint(a), false, false); return __uint_as_float(r[1]); }
DI float sum16(float v) { v += dpp<X1>(v); v += dpp<X2>(v); v += dpp<R4>(v); v += dpp<R8>(v); return v; }
DI u32 umax16(u32 v) { v = max(v, dppu<X1>(v)); v = max(v, dppu<X2>(v)); v = max(v, dppu<R4>(v)); v = max(v, dppu<R8>(v)); return v; }
DI u32 umax64(u32 v) { v = umax16(v); auto r = __builtin_amdgcn_permlane16_swap(v, v, false, false); v = max(r[0], r[1]); auto q = __builtin_amdgcn_permlane32_swap(v, v, false, false); return max(q[0], q[1]); }

DI float dot2(u32 a, u32 b, float c) { return __builtin_amdgcn_fdot2_f32_bf16(__builtin_bit_cast(bf16x2, a), __builtin_bit_cast(bf16x2, b), c, false); }
DI u32 rfl(u32 v) { return (u32)__builtin_amdgcn_readfirstlane((int)v); }
DI float rdl(float v, int l) { return __uint_as_float((u32)__builtin_amdgcn_readlane((int)__float_as_uint(v), l)); }
DI float ln_s(float var) { return bfr(sqrtf(bfr(bfr(var) + 1e-5f))); }

struct Args { const char *x, *wq, *wk, *wv, *wo, *w1, *b1, *w2, *b2, *c, *wmod; char* out; char* ws; };

__global__ __launch_bounds__(512, 2) void fused_block(Args a) {
    extern __shared__ __attribute__((aligned(16))) char lds_[];
    LAS char* lds = (LAS char*)lds_;
    const u32 ldsb = (u32)(size_t)lds;
    const int t = threadIdx.x, lane = t & 63, w = __builtin_amdgcn_readfirstlane(t >> 6);
    const int b = blockIdx.x, xcd = b & 7, j = (b >> 3) & 7, grp = xcd * 4 + (b >> 6), row0 = grp * 64, mi = b >> 3;
    const int hi = lane >> 5, g = lane >> 4, l15 = lane & 15, l31 = lane & 31, q4 = (lane & 15) >> 2, p4 = lane & 3;
    GAS char* ws = (GAS char*)a.ws;
    const __amdgpu_buffer_rsrc_t rs = __builtin_amdgcn_make_buffer_rsrc(a.ws, 0, WS_END, 0x00020000);
#define LD16(off) (*(const LAS v4u*)(lds + (off)))
#define LD8(off) (*(const LAS v2u*)(lds + (off)))
#define LDF4(off) (*(const LAS f32x4*)(lds + (off)))
#define ST16(off, v) (*(LAS v4u*)(lds + (off)) = (v))
#define ST8(off, v) (*(LAS v2u*)(lds + (off)) = (v))
#define STF4(off, v) (*(LAS f32x4*)(lds + (off)) = (v))
#define STF(off, v) (*(LAS float*)(lds + (off)) = (v))
#define LDF(off) (*(const LAS float*)(lds + (off)))
#define TR(off) __builtin_bit_cast(v2u, __builtin_amdgcn_ds_read_tr16_b64_v4i16((LAS s16x4*)(lds + (off))))
#define GL8(p) (*(const GAS v2u*)(p))
#define CLD16(off) __builtin_amdgcn_raw_buffer_load_b128(rs, (off), 0, 16)
#define CST16(off, v) __builtin_amdgcn_raw_buffer_store_b128((v), rs, (off), 0, 16)
#define CST8(off, v) __builtin_amdgcn_raw_buffer_store_b64((v), rs, (off), 0, 16)
#define AG16(d, vo, sb, imm) asm volatile("global_load_dwordx4 %0, %1, %2 offset:%3" : "=v"(d) : "v"(vo), "s"(sb), "n"(imm))
#define AG8(d, vo, sb, imm) asm volatile("global_load_dwordx2 %0, %1, %2 offset:%3" : "=v"(d) : "v"(vo), "s"(sb), "n"(imm))
#define AG2(d, vo, sb, imm) asm volatile("global_load_ushort %0, %1, %2 offset:%3" : "=v"(d) : "v"(vo), "s"(sb), "n"(imm))
#define AC16(d, vo, so, imm) asm volatile("buffer_load_dwordx4 %0, %1, %2, %3 offen offset:%4 sc1" : "=v"(d) : "v"(vo), "s"(rs), "s"(so), "n"(imm))
#define AC8(d, vo, so, imm) asm volatile("buffer_load_dwordx2 %0, %1, %2, %3 offen offset:%4 sc1" : "=v"(d) : "v"(vo), "s"(rs), "s"(so), "n"(imm))
#define DMA16(dst, vo, sb) asm volatile("s_mov_b32 m0, %0\n\ts_nop 0\n\tglobal_load_lds_dwordx4 %1, %2" :: "s"(ldsb + (dst)), "v"(vo), "s"(sb) : "m0", "memory")
#define DMA4(dst, vo, sb) asm volatile("s_mov_b32 m0, %0\n\ts_nop 0\n\tglobal_load_lds_dword %1, %2" :: "s"(ldsb + (dst)), "v"(vo), "s"(sb) : "m0", "memory")
#define DMA16I(sl, il, vo, ig, sb) do { u32 tv_; asm volatile("s_add_u32 m0, %1, %2\n\tv_add_u32 %0, %4, %3\n\tglobal_load_lds_dwordx4 %0, %5" : "=&v"(tv_) : "s"(sl), "n"(il), "v"(vo), "n"(ig), "s"(sb) : "m0", "scc", "memory"); } while (0)
#define WAITV(n, ...) asm volatile("s_waitcnt vmcnt(%c[cnt])" : __VA_ARGS__ : [cnt] "n"(n) : "memory")
#define TIE(...) asm volatile("" : __VA_ARGS__)
#define TS(i)
#define TC(i)
    auto tr2 = [&](u32 o0, u32 o1) { v2u x0 = TR(o0), x1 = TR(o1); return (v4u){x0.x, x0.y, x1.x, x1.y}; };
    TS(0); TC(30);
    if (mi >= 28) {
        u64 pc; u32 len; asm volatile("s_getpc_b64 %0\n.Lfb_code_a:\n\ts_mov_b32 %1, .Lfb_code_b-.Lfb_code_a" : "=s"(pc), "=s"(len));
        const u32 vo = min((u32)((mi - 28) * 512 + t) * 64u, len & ~3u);
        DMA4(L_DUMMY, vo, pc);
    }
    u32 Ev; { const u32 z = 0; asm volatile("global_load_dword %0, %1, %2 sc1" : "=v"(Ev) : "v"(z), "s"(a.ws)); }
    const int m3 = mi / 3, mr3 = mi - 3 * m3; const bool crit = mr3 == 0 && mi < 30, defer = mr3 != 0 && mi < 30;
    const int mcol = mr3 == 0 ? xcd * 160 + min(m3, 9) * 16 : 1280 + xcd * 320 + (min(m3, 9) * 2 + mr3 - 1) * 16;
    auto mod_load = [&](int col, v4u (&wm)[3], u32 (&cc)[3], int part) {
        const u32 mr = t >> 1, r2 = min(mr + 512u, 639u), co = col * 2 + (t & 1) * 16;
        if (part <= 0) { AG2(cc[0], mr * 2, a.c, 0); AG2(cc[1], mr * 2, a.c, 512); AG2(cc[2], r2 * 2, a.c, 0); AG16(wm[0], mr * 7680 + co, a.wmod, 0); }
        if (part < 0 || part == 1) AG16(wm[1], (mr + 256) * 7680 + co, a.wmod, 0);
        if (part < 0 || part == 2) AG16(wm[2], r2 * 7680 + co, a.wmod, 0);
    };
    auto mod_partial = [&](v4u (&wm)[3], u32 (&cc)[3]) {
        float acc[8] = {};
#pragma unroll
        for (int i = 0; i < 3; ++i) {
            const float cv = bflo(cc[i]); u32 sl = pk2(cv * __builtin_amdgcn_rcpf(1.f + __builtin_amdgcn_exp2f(cv * -1.4426950408889634f)), 0.f);
            if (i == 2 && t >= 256) sl = 0u;
            const u32 sh = sl << 16;
#pragma unroll
            for (int e = 0; e < 4; ++e) { acc[2 * e] = dot2(wm[i][e], sl, acc[2 * e]); acc[2 * e + 1] = dot2(wm[i][e], sh, acc[2 * e + 1]); }
        }
#pragma unroll
        for (int i = 0; i < 4; ++i) acc[i] = t32(acc[i], acc[i + 4]);
#pragma unroll
        for (int i = 0; i < 2; ++i) { acc[i] = t16(acc[i], acc[i + 2]); acc[i] += dpp<X2>(acc[i]); acc[i] += dpp<R4>(acc[i]); acc[i] += dpp<R8>(acc[i]); }
        if ((lane & 14) == 0) *(LAS f32x2*)(lds + L_MRED + (w * 16 + 8 * (lane & 1) + 4 * hi + 2 * (g & 1)) * 4) = (f32x2){acc[0], acc[1]};
    };
    v4u wm[3], xr[10]; u32 cc[3];
    const int tq = j >> 1, isV = j & 1; float kn2 = 0;
    const u32 l16 = lane * 16;
    if (crit) { mod_load(mcol, wm, cc, -1); __builtin_amdgcn_s_barrier(); }
    { const u32 vo = (row0 + 8 * w) * 1280 + l16, vt = (row0 + 8 * w + 2 * g) * 1280 + 1024 + l15 * 16;
#pragma unroll
      for (int i = 0; i < 8; ++i) AG16(xr[i], vo + (i / 3) * 3840, a.x, (i % 3) * 1280);
      AG16(xr[8], vt, a.x, 0); AG16(xr[9], vt, a.x, 1280); }
    v4u wm2[3]; u32 cc2[3];
    if (defer) mod_load(mcol, wm2, cc2, -1);
    const u32 slice_vo = ((u32)lane / 10u) * 1120 + l16 + j * 160 + w * 7680, slice_sl = ldsb + w * 960;
#define SLICE(W, dst, k) do { SB(); if ((k) < 13) DMA16I(slice_sl, (dst) + (k) * 7680, slice_vo, (k) * 61440, W); \
                              else if (w < 2 || (w == 2 && lane < 40)) DMA16I(slice_sl, (dst) + (k) * 7680, slice_vo, (k) * 61440, W); SB(); } while (0)
    auto mod_publish = [&](int col, u32 tg) {
        if (lane < 8) { float s0 = 0, s1 = 0;
#pragma unroll
            for (int i = 0; i < 8; ++i) { const f32x2 v = *(const LAS f32x2*)(lds + L_MRED + (i * 16 + 2 * lane) * 4); s0 += v.x; s1 += v.y; }
            CST8(WS_MODG + (col / 2 + lane) * 8, ((v2u){pk2(s0, s1), tg})); } };
    TS(23);
    if (crit) { WAITV(10, "+v"(Ev), "+v"(wm[0]), "+v"(wm[1]), "+v"(wm[2]), "+v"(cc[0]), "+v"(cc[1]), "+v"(cc[2])); mod_partial(wm, cc); bar(); if (w == 0) mod_publish(mcol, rfl(Ev) * 1664525u + 1013904223u); }
    TS(1);
    SLICE(a.wq, L_WQ, 0); SLICE(a.wq, L_WQ, 1);
    if (defer) WAITV(8); else WAITV(2);
    TIE("+v"(Ev), "+v"(xr[0]), "+v"(xr[1]), "+v"(xr[2]), "+v"(xr[3]), "+v"(xr[4]), "+v"(xr[5]), "+v"(xr[6]), "+v"(xr[7]), "+v"(xr[8]), "+v"(xr[9]));
    const u32 tag = rfl(Ev) * 1664525u + 1013904223u;
    v4u mg; const u32 mgo = min(t, 319) * 16;
    f32x2 xn[40];
    {
        float s[10], q[10];
        auto row = [&](int i) { s[i] = 0; q[i] = 0;
#pragma unroll
            for (int e = 0; e < 4; ++e) { s[i] = dot2(xr[i][e], ONE2, s[i]); q[i] = dot2(xr[i][e], xr[i][e], q[i]); } };
        row(0); row(1); SLICE(a.wq, L_WQ, 2); row(2); row(3); row(4); SLICE(a.wq, L_WQ, 3); row(5); row(6); row(7); SLICE(a.wq, L_WQ, 4); row(8); row(9);
#pragma unroll
        for (int i = 0; i < 4; ++i) { s[i] = t32(s[i], s[i + 4]); q[i] = t32(q[i], q[i + 4]); }
        SLICE(a.wq, L_WQ, 5);
        float rsd[2], nb[2];
#pragma unroll
        for (int i = 0; i < 2; ++i) { const float ss = sum16(t16(s[i], s[i + 2]) + s[8 + i]), qq = sum16(t16(q[i], q[i + 2]) + q[8 + i]);
            const float m = ss * (1.f / 640.f); rsd[i] = __builtin_amdgcn_rcpf(ln_s(fmaxf(qq * (1.f / 640.f) - m * m, 0.f))); nb[i] = -bfr(m) * rsd[i]; }
        auto nrm = [&](int i) { const float r_ = i < 8 ? rdl(rsd[i & 1], 16 * (i >> 1)) : rsd[i & 1], n_ = i < 8 ? rdl(nb[i & 1], 16 * (i >> 1)) : nb[i & 1];
#pragma unroll
            for (int e = 0; e < 4; ++e) xn[4 * i + e] = (f32x2){bflo(xr[i][e]), bfhi(xr[i][e])} * r_ + n_;
            TIE("+v"(xn[4 * i]), "+v"(xn[4 * i + 1]), "+v"(xn[4 * i + 2]), "+v"(xn[4 * i + 3])); };
        { const char* kp = isV ? a.wv : a.wk; const u32 sl = ldsb + w * 1024, vo = t * 16;
          DMA16I(sl, L_WKV, vo, 0, kp); nrm(0); DMA16I(sl, L_WKV + 8192, vo, 8192, kp); nrm(1); DMA16I(sl, L_WKV + 16384, vo, 16384, kp); if (w == 0) DMA16I(sl, L_WKV + 24576, vo, 24576, kp); }
        nrm(2); nrm(3); SLICE(a.wq, L_WQ, 6); nrm(4); nrm(5); nrm(6); SLICE(a.wq, L_WQ, 7); nrm(7); nrm(8); nrm(9); if (lane < 60) SLICE(a.wq, L_WQ, 8);
    }
    if ((w >> 1) == tq) {
#pragma unroll
        for (int i = 0; i < 10; ++i) { v4u o;
#pragma unroll
            for (int e = 0; e < 4; ++e) o[e] = pk2(xn[4 * i + e].x, xn[4 * i + e].y);
            ST16(i < 8 ? L_XK + (8 * (w & 1) + i) * HS + l16 : L_XK + (8 * (w & 1) + 2 * g + i - 8) * HS + 1024 + l15 * 16, o); } }
    if (defer) { WAITV(12, "+v"(wm2[0]), "+v"(wm2[1]), "+v"(wm2[2]), "+v"(cc2[0]), "+v"(cc2[1]), "+v"(cc2[2])); mod_partial(wm2, cc2); }
    TS(2);
    WAITV(3); bar();
    AC16(mg, mgo, WS_MODG, 0);
    TS(3);
    const int hh = w & 3, th = w >> 2, ks2 = min(w + 16, 19);
    v4u wf[40], kf[3][2]; f32x2 xk[12];
    { const u32 wb = L_WKV + (8 * g + q4) * 40 + p4 * 8;
#pragma unroll
      for (int i = 0; i < 3; ++i) { const u32 o = wb + (i < 2 ? w + 8 * i : ks2) * 1280;
          const v4u xv = LD16(L_XK + l15 * HS + g * 16 + (i < 2 ? w + 8 * i : ks2) * 64);
          kf[i][0] = tr2(o, o + 160); kf[i][1] = tr2(o + 32, o + 192);
#pragma unroll
          for (int e = 0; e < 4; ++e) xk[4 * i + e] = (f32x2){bflo(xv[e]), bfhi(xv[e])};
          TIE("+v"(xk[4 * i]), "+v"(xk[4 * i + 1]), "+v"(xk[4 * i + 2]), "+v"(xk[4 * i + 3])); } }
    const u32 wqb = L_WQ + (8 * hi + q4) * 160 + (20 * hh + 16 * (g & 1) + 4 * p4) * 2; u32 wq2 = wqb + 20 * 2560; TIE("+v"(wq2));
    auto wq_frag = [&](int ks) { const int k = ks < 27 ? ks : ks - 27; const u32 o = k < 20 ? wqb + k * 2560 : wq2 + (k - 20) * 2560; wf[ks] = tr2(o, o + 640); };
#pragma unroll
    for (int ks = 0; ks < NPRE; ++ks) wq_frag(ks);
    WAITV(0, "+v"(mg));
    if (defer && w == 7) mod_publish(mcol, tag);
    TS(26);
    if (w < 5) {
        for (u32 sp = 0; sp < SPIN_CAP; ++sp) { if (__all(mg.y == tag && mg.w == tag)) break; mg = CLD16(WS_MODG + mgo); }
        f32x4 f = {bflo(mg.x), bfhi(mg.x), bflo(mg.z), bfhi(mg.z)};
        if (t >= 160) { const u32 p0 = pk2(1.f + f.x, 1.f + f.y), p1 = pk2(1.f + f.z, 1.f + f.w); f = (f32x4){bflo(p0), bfhi(p0), bflo(p1), bfhi(p1)}; }
        STF4(L_SA + t * 16, f);
    }
    bar();
    TS(4);
    {
        f32x4 acc0 = {}, acc1 = {}, gv[6], sv[6];
#pragma unroll
        for (int i = 0; i < 6; ++i) { const u32 mo = (i < 4 ? w + 8 * (i >> 1) : ks2) * 128 + g * 32 + (i & 1) * 16; gv[i] = LDF4(L_G1 + mo); sv[i] = LDF4(L_SA + mo); }
        SB();
#pragma unroll
        for (int i = 0; i < 3; ++i) { v4u hf;
#pragma unroll
            for (int e = 0; e < 4; ++e) { const f32x4 gg = gv[2 * i + (e >> 1)], ss = sv[2 * i + (e >> 1)]; const f32x2 v = xk[4 * i + e] * (e & 1 ? gg.zw : gg.xy) + (e & 1 ? ss.zw : ss.xy); hf[e] = pk2(v.x, v.y); }
            if (i == 2 && w >= 4) hf = (v4u){};
            if (isV) { acc0 = mfma16(hf, kf[i][0], acc0); acc1 = mfma16(hf, kf[i][1], acc1); } else { acc0 = mfma16(kf[i][0], hf, acc0); acc1 = mfma16(kf[i][1], hf, acc1); } }
        STF4(L_WKV + w * 1280 + l16, acc0);
        if (isV ? l15 < 4 : g == 0) STF4(L_WKV + w * 1280 + 1024 + (isV ? 4 * g + l15 : l15) * 16, acc1);
    }
    const int key0 = row0 + 16 * tq; const u32 cb = WS_KV + (key0 >> 7) * KVC, tile = (key0 >> 5) & 3, kk = (key0 & 31) + l15, vb = cb + 4096 + tile * 1536 + (tq & 1) * 768;
    u32 kvs0 = isV ? vb + (g & 1) * 384 + l15 * 16 + 8 * (g >> 1) : cb + tile * 1024 + (kk + 32 * (g & 1)) * 16 + 8 * (g >> 1);
    u32 kvs1 = isV ? vb + (q4 & 1) * 384 + (16 + 4 * g + p4) * 16 + 8 * (q4 >> 1) : cb + 10240 + tile * 512 + kk * 16, kvr = L_WKV + g * 1280 + 1024 + l15 * 16;
    TIE("+v"(kvs0), "+v"(kvs1), "+v"(kvr));
    bar();
    TS(5);
    if (w == 0) {
        __builtin_amdgcn_s_setprio(3);
        f32x4 r0[6];
#pragma unroll
        for (int k = 0; k < 6; ++k) r0[k] = LDF4(L_WKV + k * 1280 + l16);
        SB();
        r0[0] += r0[1]; r0[2] += r0[3]; r0[1] = LDF4(L_WKV + 6 * 1280 + l16); r0[3] = LDF4(L_WKV + 7 * 1280 + l16); SB();
        r0[0] += r0[4]; r0[2] += r0[5];
        f32x4 s1 = LDF4(kvr), s2 = LDF4(kvr + 5120); SB();
        const f32x4 s0 = (r0[0] + r0[1]) + (r0[2] + r0[3]);
        const v2u p0 = {pk2(s0.x, s0.y), pk2(s0.z, s0.w)};
        CST8(kvs0, p0); SB();
        s1 += s2;
        const float u0 = t32(s1.x, s1.y), u1 = t32(s1.z, s1.w), u2 = t16(u0, u1);
        const auto uy = __builtin_amdgcn_permlane32_swap(__float_as_uint(u2), __float_as_uint(u2), false, false);
        const u32 pa = pk2(__uint_as_float(uy[0]), __uint_as_float(uy[1]));
        const auto pz = __builtin_amdgcn_permlane16_swap(pa, pa, false, false);
        v2u p1 = {pz[0], pz[1]};
        if (isV) {
            if (g) { const u32 c = p4 == 0 ? ONE2 : 0u; p1 = (v2u){c, c}; }
            if (g < 2) CST8(kvs1, p1);
        } else {
            if (g == 0) CST16(kvs1, ((v4u){p1.x, p1.y, ONE2, ONE2}));
            kn2 = dot2(p0.x, p0.x, dot2(p0.y, p0.y, 0.f));
            kn2 = t16(kn2, kn2); kn2 = t32(kn2, kn2);
            kn2 = dot2(p1.x, p1.x, dot2(p1.y, p1.y, kn2));
            kn2 = __uint_as_float(umax16(__float_as_uint(kn2)));
        }
    }
    TS(6);
    {
        const u32 hb0 = L_H + 8 * w * HS + l16, ht0 = L_H + (8 * w + 2 * g) * HS + 1024 + l15 * 16;
        f32x4 gm[2], sm[2], gt[2], st[2];
#pragma unroll
        for (int k = 0; k < 2; ++k) { gm[k] = LDF4(L_G1 + lane * 32 + k * 16); sm[k] = LDF4(L_SA + lane * 32 + k * 16); }
        SLICE(a.wq, L_WQ - 69120, 9); SLICE(a.wq, L_WQ - 69120, 10);
#pragma unroll
        for (int i = 0; i < 10; ++i) { v4u o;
            if (i == 4) {
#pragma unroll
                for (int k = 0; k < 2; ++k) { gt[k] = LDF4(L_G1 + 2048 + l15 * 32 + k * 16); st[k] = LDF4(L_SA + 2048 + l15 * 32 + k * 16); }
                SLICE(a.wq, L_WQ - 69120, 12); SLICE(a.wq, L_WQ - 69120, 13); }
            if (i == 2) SLICE(a.wq, L_WQ - 69120, 11);
#pragma unroll
            for (int e = 0; e < 4; ++e) { const f32x4 gg = i < 8 ? gm[e >> 1] : gt[e >> 1], ss = i < 8 ? sm[e >> 1] : st[e >> 1];
                const f32x2 v = xn[4 * i + e] * (e & 1 ? gg.zw : gg.xy) + (e & 1 ? ss.zw : ss.xy); o[e] = pk2(v.x, v.y); }
            if (i == KVF_I && w == 0) { WAITV(5); if (lane == 0) CST8(WS_KVF + b * 8, ((v2u){__float_as_uint(kn2), tag})); __builtin_amdgcn_s_setprio(0); TS(7); }
            ST16(i >= 8 ? ht0 + (i - 8) * HS : hb0 + i * HS, o); SB(); }
    }
    if (w == 0) WAITV(1); else WAITV(0);
    bar();
    TS(24);
    u32 ppd[3], ppv[3]; v4u ppf[3];
    const bool pp3 = w == 1 && b < 224, pp3w2 = b >= 112;
    auto pp_addr = [&](int r) { const int i = r < 2 ? b * 8 + w : 2048 + min(pp3w2 ? b - 112 : b, 111); const bool isw2 = r < 2 ? r == 1 : pp3w2;
        if (isw2) { const int ks = i / 40, nt = i - ks * 40; ppv[r] = ks * 40960 + nt * 32 + (lane >> 1) * 1280 + (lane & 1) * 16; ppd[r] = WS_W2F + (nt * 54 + ks) * 1024; }
        else { const int ks = i / 54, nt = i - ks * 54, jj = ks / 5, ii = ks - jj * 5, lr = lane >> 2;
            ppv[r] = (80 * jj + (ii < 4 ? 32 * (ii >> 1) + 8 * (ii & 1) : 64)) * 3456 + nt * 64 + (lr + (ii < 4 ? (lr & 8) : 0)) * 3456 + p4 * 16; ppd[r] = WS_W1F + (nt * 40 + ks) * 1024; }
        ppd[r] = rfl(ppd[r]); TIE("+v"(ppv[r]), "+s"(ppd[r])); };
    auto pp_load = [&](int r) { DMA16(L_PP + (r < 2 ? w + 8 * r : 16) * 1024, ppv[r], (r < 2 ? r == 1 : pp3w2) ? a.w2 : a.w1); };
    auto pp_read = [&](int r) { const bool isw2 = r < 2 ? r == 1 : pp3w2;
        const u32 o = L_PP + (r < 2 ? w + 8 * r : 16) * 1024 + (isw2 ? (16 * (g >> 1) + 4 * (g & 1) + q4) * 32 + p4 * 8 : (4 * hi + q4) * 64 + (g & 1) * 32 + p4 * 8);
        ppf[r] = tr2(o, o + (isw2 ? 256 : 512)); };
    auto pp_store = [&](int r) { __builtin_amdgcn_raw_buffer_store_b128(ppf[r], rs, l16, ppd[r], 16); };
    v4u qf0, qf1, st0; v2u st1;
    const u32 kvo0 = t * 16, kvo1 = 8192 + t * 8;
    auto kv_load = [&](int c) { AC16(st0, kvo0, WS_KV + c * KVC, 0); AC8(st1, kvo1, WS_KV + c * KVC, 0); };
    auto kv_write = [&](u32 slot) { ST16(slot + kvo0, st0); ST8(slot + kvo1, st1); };
    {
        f32x16 acc = {}; v4u hq[QD];
        const u32 hb = L_H + (32 * th + l31) * HS + hi * 16;
#pragma unroll
        for (int ks = 0; ks < QD; ++ks) hq[ks] = LD16(hb + ks * 32);
#pragma unroll
        for (int ks = 0; ks < 40; ++ks) {
            SB(); acc = mfma32(wf[ks], hq[ks % QD], acc); SB();
            if (ks + QD < 40) hq[ks % QD] = LD16(hb + (ks + QD) * 32);
            if (ks + WD >= NPRE && ks + WD < 40) wq_frag(ks + WD);
            if (ks == 1 || ks == 3 || ks == 5) pp_addr(ks >> 1);
        }
        SB();
        const float sc = 0.22360679774997896f * 1.4426950408889634f;
        qf0 = (v4u){pk2(acc[0] * sc, acc[1] * sc), pk2(acc[2] * sc, acc[3] * sc), pk2(acc[4] * sc, acc[5] * sc), pk2(acc[6] * sc, acc[7] * sc)};
        qf1 = (v4u){hi ? 0u : pk2(acc[8] * sc, acc[9] * sc), hi ? 0u : pk2(acc[10] * sc, acc[11] * sc), 0u, 0u};
        float qn2 = dot2(qf0.x, qf0.x, dot2(qf0.y, qf0.y, dot2(qf0.z, qf0.z, dot2(qf0.w, qf0.w, dot2(qf1.x, qf1.x, dot2(qf1.y, qf1.y, 0.f))))));
        qn2 = t32(qn2, qn2);
        TS(8);
        v4u kv0[9];
        if (w < 4) {
            v4u f0, f1;
            for (u32 sp = 0; sp < SPIN_CAP; ++sp) { f0 = CLD16(WS_KVF + l16); f1 = CLD16(WS_KVF + 1024 + l16);
                if (__all(f0.y == tag && f0.w == tag && f1.y == tag && f1.w == tag)) break;
                __builtin_amdgcn_s_sleep(PSLEEP); }
#pragma unroll
            for (int i = 0; i < 9; ++i) AC16(kv0[i], kvo0, WS_KV + i * 4096, 0);
            const u32 km = umax64(max(max(f0.x, f0.z), max(f1.x, f1.z)));
            if (t == 0) STF(L_A, __uint_as_float(km));
        }
        TS(9);
        bar();
        if (w < 4) {
            WAITV(5, "+v"(kv0[0]), "+v"(kv0[1]), "+v"(kv0[2]), "+v"(kv0[3]));
#pragma unroll
            for (int i = 0; i < 4; ++i) ST16(L_RING + i * 4096 + kvo0, kv0[i]);
            WAITV(0, "+v"(kv0[4]), "+v"(kv0[5]), "+v"(kv0[6]), "+v"(kv0[7]), "+v"(kv0[8]));
#pragma unroll
            for (int i = 4; i < 9; ++i) ST16(L_RING + i * 4096 + kvo0, kv0[i]);
        }
        const float kmax2 = LDF(L_A);
        const float Msh = bfr(__builtin_amdgcn_sqrtf(qn2 * kmax2) * 1.01f + 1e-30f);
        qf1.z = hi ? 0u : (pk2(-Msh, 0.f) & 0xffffu);
        TS(25);
    }
    bar();
    if (b == 0 && t == 128) __hip_atomic_store((GAS u32*)(ws + WS_E), tag, RLX_AGENT);
    TS(10); TC(28);
    const int ntile = j < 6 ? 7 : 6, nt0 = j < 6 ? 7 * j : 42 + 6 * (j - 6); const bool has_tile = w < ntile;
    const u32 w1o = WS_W1F + (nt0 + min(w, ntile - 1)) * 40960;
    v4u w1f[40];
    f32x16 oacc = {};
    {
        const u32 ak0 = L_RING + lane * 16, ak1 = L_RING + 10240 + l31 * 16, av = L_RING + 4096 + (hi * 24 + (l31 < 24 ? l31 : (l31 < 28 ? l31 - 4 : l31 - 12))) * 16;
        v4u k0 = LD16(ak0), k1 = LD16(ak1);
        f32x16 sc = mfma32(k0, qf0, (f32x16){}); sc = mfma32(k1, qf1, sc);
        k0 = LD16(ak0 + 1024); k1 = LD16(ak1 + 512);
        v4u p1 = {}, v1 = {}, pf0, pf1;
#define EX(i) __builtin_amdgcn_exp2f(sc[i])
        const int mu = t - 320; const u32 mgo2 = (t < 320 ? 1920 + 4 * t : (mu < 20 ? 1280 + 80 * j + 4 * mu : 3200 + 80 * j + 4 * (min(mu, 39) - 20))) * 4;
        auto chunk = [&](auto CC) {
            constexpr int c = decltype(CC)::value; constexpr u32 cur = (c % 3) * KVC, nxt = ((c + 1) % 3) * KVC, fre = ((c + 2) % 3) * KVC;
            constexpr int pc = c - 1, NY = pc < 6 ? 2 : 5;
#pragma unroll
            for (int i = 0; i < 4; ++i) {
                v4u p0; f32x16 sn; float e[16];
                const u32 kb = i < 2 ? cur + (i + 2) * 1024 : nxt + (i - 2) * 1024, kb1 = i < 2 ? cur + (i + 2) * 512 : nxt + (i - 2) * 512;
                sn = mfma32(k0, qf0, (f32x16){}); SB();
                const v4u v0 = LD16(cur + av + i * 1536); k0 = LD16(kb + ak0); SB();
                e[0] = EX(0); e[1] = EX(1); e[2] = EX(2); e[3] = EX(3); SB();
                oacc = mfma32(v1, p1, oacc); SB();
                v1 = LD16(cur + av + i * 1536 + 768); SB();
                e[4] = EX(4); e[5] = EX(5); SB(); p0.x = pk2(e[0], e[1]); p0.y = pk2(e[2], e[3]); SB(); e[6] = EX(6); SB();
                sn = mfma32(k1, qf1, sn); SB();
                k1 = LD16(kb1 + ak1); SB();
                e[7] = EX(7); e[8] = EX(8); SB(); p0.z = pk2(e[4], e[5]); SB(); e[9] = EX(9); SB(); p0.w = pk2(e[6], e[7]); SB(); e[10] = EX(10); e[11] = EX(11); SB();
                oacc = mfma32(v0, p0, oacc); SB();
                e[12] = EX(12); e[13] = EX(13); SB(); p1.x = pk2(e[8], e[9]); SB(); e[14] = EX(14); e[15] = EX(15); SB(); p1.y = pk2(e[10], e[11]); p1.z = pk2(e[12], e[13]); p1.w = pk2(e[14], e[15]);
                TIE("+v"(p1));
                if (i == 0) {
                    if constexpr (c > 0 && c < 14) { WAITV(NY, "+v"(st0), "+v"(st1)); kv_write(L_RING + fre); }
                    if constexpr (c == 2) { pp_read(0); pp_read(1); pp_read(2); }
                    if constexpr (c == 4) { if (t == 64) CST8(WS_WF + b * 8, ((v2u){0u, tag})); }
                    if constexpr (c == 5) { if (w == 7) { AC16(pf0, l16, WS_WF, 0); AC16(pf1, l16, WS_WF, 1024); } }
                }
                if (i == 1) {
                    if constexpr (c == 2) { pp_store(0); pp_store(1); if (pp3) pp_store(2); }
                    if constexpr (c == 14) AC16(mg, mgo2, WS_MODG, 0);
                    if constexpr (c < 13) kv_load(c + 3);
                }
                if (i == 3) { if constexpr (c == 5) { if (w == 7) {
                    WAITV(3, "+v"(pf0), "+v"(pf1));
                    for (u32 sp = 0; sp < SPIN_CAP; ++sp) { if (__all(pf0.y == tag && pf0.w == tag && pf1.y == tag && pf1.w == tag)) break;
                        __builtin_amdgcn_s_sleep(4); pf0 = CLD16(WS_WF + l16); pf1 = CLD16(WS_WF + 1024 + l16); } } } }
                if (i >= 2) {
                    if constexpr (c == 0) { pp_load(i - 2); if (i == 3 && pp3) pp_load(2); }
                    if constexpr (c >= 1 && c < 6) { if (i == 2) SLICE(a.wo, L_WO, 2 * c - 2); else SLICE(a.wo, L_WO, 2 * c - 1); }
                    if constexpr (c >= 6 && c < 14) { const int k = 4 * (c - 6) + 2 * (i - 2); AC16(w1f[k], l16, w1o + (k & ~3) * 1024, (k & 3) * 1024); AC16(w1f[k + 1], l16, w1o + (k & ~3) * 1024, ((k + 1) & 3) * 1024); }
                    if constexpr (c >= 6 && c < 10) { if (i == 2) SLICE(a.wo, L_WO, c + 4); }
                    if constexpr (c >= 9) { if (i == 3 && lane < TOUCH_N) DMA4(L_DUMMY, WS_W2F + min((u32)(((mi * 7 + c - 9) * 8 + w) * TOUCH_N + lane), 2160u * 1024u / TOUCH_S - 1) * TOUCH_S, a.ws); }
                }
                SB();
                sc = sn;
            }
            bar();
        };
        chunk(IC<0>{}); chunk(IC<1>{}); chunk(IC<2>{}); chunk(IC<3>{}); chunk(IC<4>{}); chunk(IC<5>{}); chunk(IC<6>{}); chunk(IC<7>{});
        chunk(IC<8>{}); chunk(IC<9>{}); chunk(IC<10>{}); chunk(IC<11>{}); chunk(IC<12>{}); chunk(IC<13>{}); chunk(IC<14>{}); chunk(IC<15>{});
        oacc = mfma32(v1, p1, oacc);
    }
    TS(11); TC(29);
    {
    int t2 = threadIdx.x; TIE("+v"(t2));
    const int t = t2, lane = t & 63, hi = lane >> 5, g = lane >> 4, l15 = lane & 15, l31 = lane & 31, q4 = (lane & 15) >> 2, p4 = lane & 3; const u32 l16 = lane * 16;
    float lsum = hi32(oacc[8]);
    if (__any(!(lsum >= 1e-12f && lsum < 3e38f))) {
        qf1.z = 0u;
        const u32 ak0 = WS_KV + lane * 16, ak1 = WS_KV + 10240 + l31 * 16, av = WS_KV + 4096 + (hi * 24 + (l31 < 24 ? l31 : 20)) * 16;
        float mx = -__builtin_inff();
        for (int tI = 0; tI < 64; ++tI) { const u32 cb = (tI >> 2) * KVC, i = tI & 3;
            const v4u k0 = CLD16(cb + ak0 + i * 1024), k1 = CLD16(cb + ak1 + i * 512);
            f32x16 s = mfma32(k0, qf0, (f32x16){}); s = mfma32(k1, qf1, s);
#pragma unroll
            for (int r = 0; r < 16; ++r) mx = fmaxf(mx, s[r]); }
        mx = fmaxf(mx, __shfl_xor(mx, 32));
        oacc = (f32x16){};
        for (int tI = 0; tI < 64; ++tI) { const u32 cb = (tI >> 2) * KVC, i = tI & 3;
            const v4u k0 = CLD16(cb + ak0 + i * 1024), k1 = CLD16(cb + ak1 + i * 512);
            const v4u v0 = CLD16(cb + av + i * 1536), v1 = CLD16(cb + av + i * 1536 + 768);
            f32x16 s = mfma32(k0, qf0, (f32x16){}); s = mfma32(k1, qf1, s);
            v4u p0, p1;
#pragma unroll
            for (int e = 0; e < 4; ++e) { p0[e] = pk2(__builtin_amdgcn_exp2f(s[2 * e] - mx), __builtin_amdgcn_exp2f(s[2 * e + 1] - mx));
                                          p1[e] = pk2(__builtin_amdgcn_exp2f(s[8 + 2 * e] - mx), __builtin_amdgcn_exp2f(s[9 + 2 * e] - mx)); }
            oacc = mfma32(v0, p0, oacc); oacc = mfma32(v1, p1, oacc); }
        lsum = hi32(oacc[8]);
    }
    const u32 gfl = WS_GF + grp * 1024;
    const int mp = w >> 1, par = w & 1;
    v2u xres[3]; u32 b1v;
    {
        const float il = __builtin_amdgcn_rcpf(lsum); const int mt = 2 * th + (l31 >> 4);
        const v4u o0 = {pk2(oacc[0] * il, oacc[1] * il), pk2(oacc[2] * il, oacc[3] * il), pk2(oacc[4] * il, oacc[5] * il), pk2(oacc[6] * il, oacc[7] * il)};
        const u32 ob = WS_O + grp * 81920 + mt * 20480 + l15 * 16;
        CST16(ob + (2 * j + (hh >> 1)) * 1024 + (2 * (hh & 1) + hi) * 256, o0);
        const int ci = 64 + 2 * j + (hh >> 1);
        if (!hi) CST8(ob + ci * 256 + 8 * (hh & 1), ((v2u){pk2(oacc[8] * il, oacc[9] * il), pk2(oacc[10] * il, oacc[11] * il)}));
        SB();
        const u32 xo = (row0 + 16 * mp + l15) * 1280 + j * 160 + g * 8;
        AG8(xres[0], xo + par * 64, a.x, 0); AG8(xres[1], xo + par * 64, a.x, 32); AG8(xres[2], xo, a.x, 128);
        AG2(b1v, ((nt0 + min(w, ntile - 1)) * 32 + l31) * 2, a.b1, 0);
    }
    {
        WAITV(6, "+v"(mg));
        if (t == 511) STF(L_MRED, 0.f);
        const int mu = t - 320; const u32 mgo2 = (t < 320 ? 1920 + 4 * t : (mu < 20 ? 1280 + 80 * j + 4 * mu : 3200 + 80 * j + 4 * (min(mu, 39) - 20))) * 4;
                        if (w < 6) {
                            for (u32 sp = 0; sp < SPIN_CAP; ++sp) { if (__all(mg.y == tag && mg.w == tag)) break; mg = CLD16(WS_MODG + mgo2); }
                            v2u v = {mg.x, mg.z}; f32x4 f = {bflo(v.x), bfhi(v.x), bflo(v.y), bfhi(v.y)};
                            if (w < 5) {
                                const int isg = t >= 160, tp = t - 160 * isg, jj = (tp * 205) >> 12, r4 = tp - jj * 20;
                                if (isg) { v = (v2u){pk2(1.f + f.x, 1.f + f.y), pk2(1.f + f.z, 1.f + f.w)}; f = (f32x4){bflo(v.x), bfhi(v.x), bflo(v.y), bfhi(v.y)}; }
                                const int o = r4 < 16 ? (r4 >> 3) * 64 + (r4 & 3) * 16 + (r4 & 4) * 2 : 128 + (r4 & 1) * 16 + (r4 & 2) * 4;
                                ST8(L_AUX + (isg ? 0 : 1280) + jj * 160 + o, v);
                                if (jj == j) { if (isg) STF4(L_OWN + 320 + r4 * 16, f); ST8(L_OWN + (isg ? 960 : 1120) + r4 * 8, v); }
                            } else if (mu < 40) STF4(L_OWN + (mu < 20 ? mu * 16 : 640 + (mu - 20) * 16), f);
                        } else if (t < 464) ST16(L_AUX + 2560 + (t - 384) * 16, ((v4u){ONE2, ONE2, ONE2, ONE2}));
    }
#define W1T(k) "+v"(w1f[k]), "+v"(w1f[k + 1]), "+v"(w1f[k + 2]), "+v"(w1f[k + 3]), "+v"(w1f[k + 4]), "+v"(w1f[k + 5]), "+v"(w1f[k + 6]), "+v"(w1f[k + 7])
    WAITV(4, W1T(0), W1T(8)); TIE(W1T(16), W1T(24));
    if (lane < 12) DMA4(L_DUMMY, WS_O + grp * 81920 + (2 * th + (lane & 1)) * 20480 + (lane < 8 ? (2 * j + (hh >> 1)) * 1024 + (hh & 1) * 512 + (lane >> 1) * 128 : (64 + 2 * j + (hh >> 1)) * 256 + (lane & 2) * 64), a.ws);
    bar();
    if (t == 0) __hip_atomic_store((GAS u32*)(ws + gfl + j * 128), tag, RLX_AGENT);
    TS(12);
    auto wait_flags = [&](int phase, int first, int n) {
        for (u32 sp = 0; sp < SPIN_CAP; ++sp) { const u32 f = __builtin_amdgcn_raw_buffer_load_b32(rs, gfl + phase * 32768 + (first + (lane & (n - 1))) * 128, 0, 16);
            if (__all(f == tag)) break; __builtin_amdgcn_s_sleep(1); } };
    const u32 ab = L_AUX + min(l31, 2) * 1280 + hi * 16;
    float up[3];
    auto ua_part = [&](auto K0, auto K1) { constexpr int k0 = decltype(K0)::value, k1 = decltype(K1)::value; f32x16 ua = {}; v4u af[4];
#pragma unroll
        for (int ks = k0; ks < k0 + 4; ++ks) af[ks & 3] = LD16(ab + ks * 32);
#pragma unroll
        for (int ks = k0; ks < k1; ++ks) { SB(); ua = mfma32(af[ks & 3], w1f[ks], ua); SB(); if (ks + 4 < k1) af[ks & 3] = LD16(ab + (ks + 4) * 32); }
#pragma unroll
        for (int i = 0; i < 3; ++i) up[i] = k0 ? up[i] + ua[i] : ua[i];
        TIE("+v"(up[0]), "+v"(up[1]), "+v"(up[2])); };
    ua_part(IC<0>{}, IC<UA1>{});
    u32 x1p[6];
    {
        v4u xf[10], wr[WOD]; f32x4 acc[5] = {};
        const u32 wm_ = L_WO + (320 * par + 20 * (g >> 1) + 4 * (g & 1) + q4) * 160 + p4 * 8, wl_ = L_WO + (320 * par + 40 * g + 16 + q4) * 160 + p4 * 8;
        auto wo_frag = [&](int s_) { const int i = s_ / 5, nt = s_ % 5; const u32 o = i < 8 ? wm_ + i * 6400 + nt * 32 : wl_ + (i - 8) * 25600 + nt * 32; wr[s_ % WOD] = tr2(o, o + (i < 8 ? 1280 : 3200)); };
#pragma unroll
        for (int s_ = 0; s_ < WOD; ++s_) wo_frag(s_);
        SB();
        wait_flags(0, 4 * par, 4);
        TS(13);
        { const u32 ob = WS_O + grp * 81920 + mp * 20480 + par * 8192, ot = WS_O + grp * 81920 + mp * 20480 + 16384 + par * 2048;
#pragma unroll
          for (int i = 0; i < 10; ++i) { if (i < 8) AC16(xf[i], l16, ob + (i & 4) * 1024, (i & 3) * 1024); else AC16(xf[i], l16, ot, (i - 8) * 1024); } }
#pragma unroll
        for (int s_ = 0; s_ < 50; ++s_) { const int i = s_ / 5, nt = s_ % 5;
            if (nt == 0) WAITV(9 - i, "+v"(xf[i]));
            SB(); acc[nt] = mfma16(wr[s_ % WOD], xf[i], acc[nt]); SB();
            if (s_ + WOD < 50) wo_frag(s_ + WOD); }
        SB(); TIE("+v"(xres[0]), "+v"(xres[1]), "+v"(xres[2]), "+v"(b1v)); SB();
        const u32 rb = L_WRED + w * 3072 + l16;
        STF4(rb, par ? acc[0] : acc[2]); STF4(rb + 1024, par ? acc[1] : acc[3]); STF4(rb + 2048, acc[4]);
        f32x4 ga[3], g2[3]; v2u g2p[3], sbp[3];
#pragma unroll
        for (int i = 0; i < 3; ++i) { const u32 co = (i < 2 ? par * 128 + i * 64 : 256) + g * 16;
            ga[i] = LDF4(L_OWN + co); g2[i] = LDF4(L_OWN + 320 + co); g2p[i] = LD8(L_OWN + 960 + co / 2); sbp[i] = LD8(L_OWN + 1120 + co / 2); }
        u32 xb = WS_XG + grp * 81920 + (mp >> 1) * 40960 + (16 * (mp & 1) + l15 + 32 * (g & 1)) * 16 + 5 * j * 1024, xb0 = xb + (2 * par + (g >> 1)) * 1024, xb1 = xb + 4096 + 8 * (g >> 1);
        u32 sto = WS_ST + (grp * 8 + j) * 4096 + (par * 3 + g) * 512 + (16 * mp + l15) * 8;
        f32x4 r[3]; r[0] = par ? acc[2] : acc[0]; r[1] = par ? acc[3] : acc[1]; r[2] = acc[4];
        TIE("+v"(xb0), "+v"(xb1), "+v"(sto), "+v"(r[0]), "+v"(r[1]));
        bar();
        TS(22);
        const u32 pb = L_WRED + (w ^ 1) * 3072 + l16;
        r[0] += LDF4(pb); r[1] += LDF4(pb + 1024); r[2] += LDF4(pb + 2048);
        u32 xg[6];
#pragma unroll
        for (int i = 0; i < 3; ++i) {
            x1p[2 * i] = pk2(r[i][0] * ga[i][0] + bflo(xres[i].x), r[i][1] * ga[i][1] + bfhi(xres[i].x)); x1p[2 * i + 1] = pk2(r[i][2] * ga[i][2] + bflo(xres[i].y), r[i][3] * ga[i][3] + bfhi(xres[i].y));
            xg[2 * i] = pk2(bflo(x1p[2 * i]) * g2[i][0], bfhi(x1p[2 * i]) * g2[i][1]); xg[2 * i + 1] = pk2(bflo(x1p[2 * i + 1]) * g2[i][2], bfhi(x1p[2 * i + 1]) * g2[i][3]);
        }
        CST16(xb0, ((v4u){xg[0], xg[1], xg[2], xg[3]}));
        if (hi == par) CST8(xb1, ((v2u){xg[4], xg[5]}));
        float sm[6] = {};
#pragma unroll
        for (int i = 0; i < 6; ++i) { const bool off = i >= 4 && hi != par; const u32 xa = off ? 0u : x1p[i], ya = off ? 0u : xg[i];
            sm[0] = dot2(xa, ONE2, sm[0]); sm[1] = dot2(xa, xa, sm[1]); sm[2] = dot2(ya, ONE2, sm[2]); sm[3] = dot2(ya, ya, sm[3]);
            sm[4] = dot2(ya, g2p[i >> 1][i & 1], sm[4]); sm[5] = dot2(ya, sbp[i >> 1][i & 1], sm[5]); }
        const float e0 = t32(sm[0], sm[4]), e1 = t32(sm[1], sm[5]), e2 = t32(sm[2], sm[2]), e3 = t32(sm[3], sm[3]);
        const float f0 = t16(e0, e2), f1 = t16(e1, e3);
        if (g < 3) CST8(sto, ((v2u){__float_as_uint(f0), __float_as_uint(f1)}));
        SB();
#pragma unroll
        for (int k = 32; k < 40; ++k) AC16(w1f[k], l16, w1o + (k & ~3) * 1024, (k & 3) * 1024);
        WAITV(8);
        if ((lane & 7) == 0) DMA4(L_DUMMY, xb0, a.ws);
        if ((lane & 7) == 0) DMA4(L_DUMMY, (lane & 8) && g < 3 ? sto : (xb1 & ~255u) + hi * 128, a.ws);
    }
    bar();
    if (t == 0) __hip_atomic_store((GAS u32*)(ws + gfl + 32768 + j * 128), tag, RLX_AGENT);
    TS(14);
    v4u wex;
    {
        ua_part(IC<UA1>{}, IC<32>{});
        WAITV(2 * TOUCH_OWN, W1T(32));
        ua_part(IC<32>{}, IC<40>{});
        {
            const u32 h0 = pk2(up[0], up[1]), h2 = pk2(up[2], 0.f);
            const u32 l0 = pk2(up[0] - bflo(h0), up[1] - bfhi(h0)), l2 = pk2(up[2] - bflo(h2), 0.f);
            wex.x = hi ? (b1v | (b1v << 16)) : ((h0 & 0xffffu) | (l0 << 16)); wex.y = hi ? 0u : ((h0 >> 16) | (l0 & 0xffff0000u));
            wex.z = hi ? 0u : ((h2 & 0xffffu) | (l2 << 16)); wex.w = hi ? 0u : (h2 & 0xffffu);
            TIE("+v"(wex));
        }
    }
    float cst[5];
    if (w == 7) {
#pragma unroll
        for (int i = 0; i < 5; ++i) cst[i] = 0;
#pragma unroll
        for (int r = 0; r < 2; ++r) { const int ch = lane + 64 * r; v4u gv = LD16(L_AUX + min(ch, 79) * 16), sv = LD16(L_AUX + 1280 + min(ch, 79) * 16);
#pragma unroll
            for (int e = 0; e < 4; ++e) { const u32 g_ = ch < 80 ? gv[e] : 0u, s_ = ch < 80 ? sv[e] : 0u;
                cst[0] = dot2(g_, ONE2, cst[0]); cst[1] = dot2(g_, g_, cst[1]); cst[2] = dot2(g_, s_, cst[2]); cst[3] = dot2(s_, ONE2, cst[3]); cst[4] = dot2(s_, s_, cst[4]); } }
#pragma unroll
        for (int i = 0; i < 5; ++i) { cst[i] = sum16(cst[i]); cst[i] = t16(cst[i], cst[i]); cst[i] = t32(cst[i], cst[i]); }
    }
    wait_flags(1, w, 1);
    TS(15);
    {
        v4u xi[13]; const u32 xb = WS_XG + grp * 81920 + 5 * w * 1024, sbo = WS_ST + (grp * 8 + w) * 4096;
#pragma unroll
        for (int i = 0; i < 3; ++i) AC16(xi[10 + i], l16, sbo, i * 1024);
#pragma unroll
        for (int i = 0; i < 10; ++i) AC16(xi[i], l16, xb + (i / 5) * 40960 + (i % 5 == 4 ? 4096 : 0), (i % 5 & 3) * 1024);
        WAITV(10, "+v"(xi[10]), "+v"(xi[11]), "+v"(xi[12]));
#pragma unroll
        for (int i = 0; i < 3; ++i) ST16(L_STP + w * 3072 + i * 1024 + l16, xi[10 + i]);
#pragma unroll
        for (int i = 0; i < 10; ++i) { WAITV(9 - i, "+v"(xi[i])); ST16(L_X + (i / 5) * 40960 + (5 * w + i % 5) * 1024 + l16, xi[i]); }
    }
    bar();
    TS(16);
    const int ks0 = w < 6 ? 7 * w : 42 + 6 * (w - 6), nks = w < 6 ? 7 : 6;
    v2u b2v[3];
#define AC16R(d, vo, so, imm) asm volatile("buffer_load_dwordx4 %0, %1, %2, %3 offen offset:%4 sc1" : "+v"(d) : "v"(vo), "s"(rs), "s"(so), "n"(imm))
    {
        const u32 w2o = WS_W2F + (5 * j * 54 + ks0) * 1024, w2l = WS_W2F + (5 * j * 54 + ks0 + nks - 1) * 1024;
        auto w2_load = [&](int k) { const int i = k / 5, nt = k % 5; if (i < 6) AC16R(w1f[k], l16, w2o + nt * 55296 + (i & ~3) * 1024, (i & 3) * 1024); else AC16R(w1f[k], l16, w2l + nt * 55296, 0); };
        f32x16 a0 = {}, a1 = {};
        if (w == 7) {
            __builtin_amdgcn_s_setprio(3);
            f32x2 us[3] = {};
#pragma unroll
            for (int i = 0; i < 48; ++i) us[i % 3] += *(const LAS f32x2*)(lds + L_STP + i * 512 + lane * 8);
            const f32x4 u0 = {us[0].x, us[0].y, us[1].x, us[1].y}; const f32x2 u1 = us[2];
            const float in = 1.f / 640.f, m1 = u0.x * in, m1b = bfr(m1), s1 = ln_s(fmaxf(u0.y * in - m1 * m1, 0.f)), r1 = 1.f / s1;
            const float m2 = (r1 * (u0.z - m1b * cst[0]) + cst[3]) * in;
            const float q2 = (r1 * r1 * (u0.w - 2.f * m1b * u1.x + m1b * m1b * cst[1]) + 2.f * r1 * (u1.y - m1b * cst[2]) + cst[4]) * in;
            const float s2 = ln_s(fmaxf(q2 - m2 * m2, 0.f)), bw = -bfr(m2) * s1, gm = s1 * s2;
            const u32 bh = pk2(bw, gm), bl = pk2(bw - bflo(bh), gm - bfhi(bh));
            const u32 xe = L_XE + hi * 1024 + l31 * 16;
            ST16(xe, ((v4u){pk2(-m1b, -m1b), pk2(s1, s1), (bh & 0xffffu) | (bh << 16), bl & 0xffffu}));
            ST16(xe + 512, ((v4u){(bh >> 16) | (bl & 0xffff0000u), 0u, 0u, 0u}));
            STF(L_A + lane * 4, 1.f / gm);
            asm volatile("" ::: "memory"); STF(L_MRED, 1.f);
            __builtin_amdgcn_s_setprio(0);
        }
        {
            u32 xa = L_X + l16, xb2 = L_X + 40960 + l16; TIE("+v"(xb2));
            v4u x0[3], x1[3];
#pragma unroll
            for (int ks = 0; ks < 2; ++ks) { x0[ks] = LD16(xa + ks * 1024); x1[ks] = LD16(xb2 + ks * 1024); }
#pragma unroll
            for (int ks = 0; ks < 40; ++ks) {
                if (ks + 2 < 40) { x0[(ks + 2) % 3] = LD16(xa + (ks + 2) * 1024); x1[(ks + 2) % 3] = LD16(xb2 + (ks + 2) * 1024); }
                SB();
                if (has_tile) { a0 = mfma32(w1f[ks], x0[ks % 3], a0); a1 = mfma32(w1f[ks], x1[ks % 3], a1); }
                SB();
                if (ks >= 1 && ks < 36) w2_load(ks - 1); }
        }
        { const u32 bo = (80 * j + par * 32 + g * 4) * 2; AG8(b2v[0], bo, a.b2, 0); AG8(b2v[1], bo, a.b2, 32); AG8(b2v[2], (80 * j + 64 + g * 4) * 2, a.b2, 0); }
        TS(17);
        u32 hb = WS_H + grp * 221184 + ((l31 >> 4) * 54 + nt0 + min(w, ntile - 1)) * 1024 + (l15 + 16 * hi) * 16;
        if (has_tile) {
            for (u32 sp = 0; sp < SPIN_CAP; ++sp) { if (*(volatile LAS float*)(lds + L_MRED) == 1.f) break; __builtin_amdgcn_s_sleep(1); }
            asm volatile("" ::: "memory");
            a0 = mfma32(wex, LD16(L_XE + l16), a0); a1 = mfma32(wex, LD16(L_XE + 1024 + l16), a1);
            const float A0 = LDF(L_A + l31 * 4), A1 = LDF(L_A + 128 + l31 * 4);
            const float k1 = -2.f * 0.7978845608028654f * 1.4426950408889634f, k3 = k1 * 0.044715f;
            auto gelu = [&](float z) { return z * __builtin_amdgcn_rcpf(1.f + __builtin_amdgcn_exp2f(z * (k1 + k3 * z * z))); };
#pragma unroll
            for (int tt = 0; tt < 2; ++tt)
#pragma unroll
                for (int hf = 0; hf < 2; ++hf) { v4u o; const float A = tt ? A1 : A0;
#pragma unroll
                    for (int e = 0; e < 4; ++e) { const int r = 8 * hf + 2 * e; o[e] = pk2(gelu(A * (tt ? a1[r] : a0[r])), gelu(A * (tt ? a1[r + 1] : a0[r + 1]))); }
                    CST16(hb + tt * 108 * 1024 + hf * 512, o); }
        }
        WAITV(0, W1T(0), W1T(8), "+v"(b2v[0]), "+v"(b2v[1]), "+v"(b2v[2])); TIE(W1T(16), W1T(24), "+v"(w1f[32]), "+v"(w1f[33]), "+v"(w1f[34]));
        if (has_tile && (lane & 7) < 4) DMA4(L_DUMMY, (hb & ~127u) + (lane & 1) * 512 + (lane & 2) * 54 * 1024, a.ws);
    }
    bar();
    if (t == 0) __hip_atomic_store((GAS u32*)(ws + gfl + 65536 + j * 128), tag, RLX_AGENT);
    TS(18);
    wait_flags(2, w, 1);
    TS(19);
    {
        f32x4 own[3]; v4u hf[14];
        const u32 hb = WS_H + grp * 221184 + ks0 * 1024;
        auto h_load = [&](int m, int i) { const u32 o = hb + ((mp + 1 + m) & 3) * 55296; v4u& d = hf[(m & 1) * 7 + i];
            if (i < 6) AC16(d, l16, o + (i & 4) * 1024, (i & 3) * 1024); else AC16(d, l16, o + (nks - 1) * 1024, 0); };
#pragma unroll
        for (int k = 0; k < 14; ++k) h_load(k / 7, k % 7);
#pragma unroll
        for (int m = 0; m < 4; ++m) { const int mt = (mp + 1 + m) & 3;
            f32x4 acc[5] = {};
#pragma unroll
            for (int i = 0; i < 7; ++i) { v4u& d = hf[(m & 1) * 7 + i];
                WAITV(m < 2 ? 13 : 27 - 7 * m - i, "+v"(d));
                if (i < 6 || nks == 7) {
#pragma unroll
                    for (int nt = 0; nt < 5; ++nt) acc[nt] = mfma16(w1f[i * 5 + nt], d, acc[nt]); }
                SB();
                if (m < 2) h_load(m + 2, i); }
            if (m == 3) {
                const u32 pb = L_PART + mt * 35840 + 6 * 5120 + l16;
                STF4(pb + (par ? 0 : 2048), par ? acc[0] : acc[2]); STF4(pb + (par ? 1024 : 3072), par ? acc[1] : acc[3]);
                if (hi != par) STF4(pb + 4096, acc[4]);
                own[0] = par ? acc[2] : acc[0]; own[1] = par ? acc[3] : acc[1]; own[2] = acc[4];
            } else {
                const u32 pb = L_PART + mt * 35840 + ((w - 2 * mt - 2) & 7) * 5120 + l16;
#pragma unroll
                for (int nt = 0; nt < 5; ++nt) STF4(pb + nt * 1024, acc[nt]);
            }
        }
        f32x4 gb[3], pr[3][7];
#pragma unroll
        for (int i = 0; i < 3; ++i) gb[i] = LDF4(L_OWN + 640 + ((i < 2 ? par * 32 + i * 16 : 64) + g * 4) * 4);
        bar();
        TS(20);
#pragma unroll
        for (int i = 0; i < 3; ++i)
#pragma unroll
            for (int k = 0; k < 7; ++k) pr[i][k] = LDF4(L_PART + mp * 35840 + (i < 2 ? 2 * par + i : 4) * 1024 + l16 + k * 5120);
        SB();
        GAS char* op = (GAS char*)a.out + (size_t)(row0 + 16 * mp + l15) * 1280 + j * 160 + g * 8;
#pragma unroll
        for (int i = 0; i < 3; ++i) {
            const f32x4 s = ((own[i] + pr[i][0]) + (pr[i][1] + pr[i][2])) + ((pr[i][3] + pr[i][4]) + (pr[i][5] + pr[i][6])); const v2u bb = b2v[i];
            const float o0 = bflo(x1p[2 * i]) + (s.x + bflo(bb.x)) * gb[i].x, o1 = bfhi(x1p[2 * i]) + (s.y + bfhi(bb.x)) * gb[i].y;
            const float o2 = bflo(x1p[2 * i + 1]) + (s.z + bflo(bb.y)) * gb[i].z, o3 = bfhi(x1p[2 * i + 1]) + (s.w + bfhi(bb.y)) * gb[i].w;
            if (i < 2 || hi == par) *(GAS v2u*)(op + (i < 2 ? par * 64 + i * 32 : 128)) = (v2u){pk2(o0, o1), pk2(o2, o3)};
        }
    }
    }
    TS(21); TC(31);
    asm volatile(".Lfb_code_b:");
}

extern "C" void kernel_launch(void* const* d_in, const int* in_sizes, int n_in, void* d_out, int out_size, void* d_ws, size_t ws_size, hipStream_t stream) {
    Args a;
    a.x = (const char*)d_in[0]; a.wq = (const char*)d_in[1]; a.wk = (const char*)d_in[2]; a.wv = (const char*)d_in[3]; a.wo = (const char*)d_in[4];
    a.w1 = (const char*)d_in[6]; a.b1 = (const char*)d_in[7]; a.w2 = (const char*)d_in[8]; a.b2 = (const char*)d_in[9]; a.c = (const char*)d_in[10]; a.wmod = (const char*)d_in[11];
    a.out = (char*)d_out; a.ws = (char*)d_ws;
    (void)hipFuncSetAttribute((const void*)fused_block, hipFuncAttributeMaxDynamicSharedMemorySize, L_TOTAL);
    hipLaunchKernelGGL(fused_block, dim3(256), dim3(512), L_TOTAL, stream, a);
}
```

```cpp
#define OAUX 16
#include <hip/hip_runtime.h>
#include <hip/hip_bf16.h>
typedef unsigned u32; typedef unsigned long long u64; typedef unsigned short u16;
typedef u32 v4u __attribute__((ext_vector_type(4)));
typedef u32 v2u __attribute__((ext_vector_type(2)));
typedef float f32x2 __attribute__((ext_vector_type(2)));
typedef float f32x4 __attribute__((ext_vector_type(4)));
typedef float f32x16 __attribute__((ext_vector_type(16)));
typedef __bf16 bf16x8 __attribute__((ext_vector_type(8)));
typedef __bf16 bf16x2 __attribute__((ext_vector_type(2)));
typedef short s16x4 __attribute__((ext_vector_type(4)));
#define GAS __attribute__((address_space(1)))
#define LAS __attribute__((address_space(3)))
#define RLX_AGENT __ATOMIC_RELAXED, __HIP_MEMORY_SCOPE_AGENT
#define DI __device__ __forceinline__
#define SB() __builtin_amdgcn_sched_barrier(0)

constexpr u32 KVC = 12288;
constexpr u32 WS_E = 0, WS_MODG = 1024, WS_KVF = 16384, WS_WF = 18432, WS_GF = 32768, WS_KV = 163840, WS_O = WS_KV + 16 * KVC,
              WS_XG = WS_O + 32 * 81920, WS_ST = WS_XG + 32 * 81920, WS_H = WS_ST + 32 * 8 * 64 * 64, WS_W1F = WS_H + 32 * 221184,
              WS_W2F = WS_W1F + 2160 * 1024, WS_END = WS_W2F + 2160 * 1024, WS_DBG = 20u << 20;
constexpr u32 L_H = 0, L_WQ = 82944, L_WKV = 0, L_XK = 25600, L_PP = 139264, L_WO = 0, L_RING = 102400, L_WRED = 102400, L_X = 0, L_XE = 81920, L_STP = 126976, L_PART = 0,
              L_SA = 152064, L_G1 = 154624, L_AUX = 157184, L_MRED = 161024, L_A = 161536, L_OWN = 161792, L_DUMMY = 163328, L_TOTAL = 163840;
constexpr int HS = 1296;
constexpr int NPRE = 13;
#define QD 6
#define WD 6
#define KVF_I 6
#define POLLW 3
#define PSLEEP 4
#define TOUCH_OWN 1
#define UA1 24
#define WOD 8
#define TOUCH_S 128u
constexpr u32 TOUCH_N = 1280 / TOUCH_S;
constexpr u32 SPIN_CAP = 400000, ONE2 = 0x3F803F80u;
template <int N> struct IC { static constexpr int value = N; };

DI float bflo(u32 v) { return __uint_as_float(v << 16); }
DI float bfhi(u32 v) { return __uint_as_float(v & 0xffff0000u); }
DI u32 pk2(float a, float b) { f32x2 v = {a, b}; return __builtin_bit_cast(u32, __builtin_convertvector(v, bf16x2)); }
DI float bfr(float a) { return bflo(pk2(a, a)); }
DI f32x16 mfma32(v4u a, v4u b, f32x16 c) { return __builtin_amdgcn_mfma_f32_32x32x16_bf16(__builtin_bit_cast(bf16x8, a), __builtin_bit_cast(bf16x8, b), c, 0, 0, 0); }
DI f32x4 mfma16(v4u a, v4u b, f32x4 c) { return __builtin_amdgcn_mfma_f32_16x16x32_bf16(__builtin_bit_cast(bf16x8, a), __builtin_bit_cast(bf16x8, b), c, 0, 0, 0); }
DI void bar() { asm volatile("s_waitcnt lgkmcnt(0)" ::: "memory"); __builtin_amdgcn_s_barrier(); asm volatile("" ::: "memory"); }
DI void vm0() { asm volatile("s_waitcnt vmcnt(0)" ::: "memory"); }
template <int CTRL> DI u32 dppu(u32 v) { return (u32)__builtin_amdgcn_update_dpp(0, (int)v, CTRL, 0xf, 0xf, true); }
template <int CTRL> DI float dpp(float v) { return __uint_as_float(dppu<CTRL>(__float_as_uint(v))); }
constexpr int X1 = 0xB1, X2 = 0x4E, HM = 0x141, R4 = 0x124, R8 = 0x128;
DI float t16(float a, float b) { auto r = __builtin_amdgcn_permlane16_swap(__float_as_uint(a), __float_as_uint(b), false, false); return __uint_as_float(r[0]) + __uint_as_float(r[1]); }
DI float t32(float a, float b) { auto r = __builtin_amdgcn_permlane32_swap(__float_as_uint(a), __float_as_uint(b), false, false); return __uint_as_float(r[0]) + __uint_as_float(r[1]); }
DI float hi32(float a) { auto r = __builtin_amdgcn_permlane32_swap(__float_as_uint(a), __float_as_uint(a), false, false); return __uint_as_float(r[1]); }
DI float sum16(float v) { v += dpp<X1>(v); v += dpp<X2>(v); v += dpp<R4>(v); v += dpp<R8>(v); return v; }
DI u32 umax16(u32 v) { v = max(v, dppu<X1>(v)); v = max(v, dppu<X2>(v)); v = max(v, dppu<R4>(v)); v = max(v, dppu<R8>(v)); return v; }
DI u32 umax64(u32 v) { v = umax16(v); auto r = __builtin_amdgcn_permlane16_swap(v, v, false, false); v = max(r[0], r[1]); auto q = __builtin_amdgcn_permlane32_swap(v, v, false, false); return max(q[0], q[1]); }

DI float dot2(u32 a, u32 b, float c) { return __builtin_amdgcn_fdot2_f32_bf16(__builtin_bit_cast(bf16x2, a), __builtin_bit_cast(bf16x2, b), c, false); }
DI u32 rfl(u32 v) { return (u32)__builtin_amdgcn_readfirstlane((int)v); }
DI float rdl(float v, int l) { return __uint_as_float((u32)__builtin_amdgcn_readlane((int)__float_as_uint(v), l)); }
DI float ln_s(float var) { return bfr(sqrtf(bfr(bfr(var) + 1e-5f))); }

struct Args { const char *x, *wq, *wk, *wv, *wo, *w1, *b1, *w2, *b2, *c, *wmod; char* out; char* ws; };

__global__ __launch_bounds__(512, 2) void fused_block(Args a) {
    extern __shared__ __attribute__((aligned(16))) char lds_[];
    LAS char* lds = (LAS char*)lds_;
    const u32 ldsb = (u32)(size_t)lds;
    const int t = threadIdx.x, lane = t & 63, w = __builtin_amdgcn_readfirstlane(t >> 6);
    const int b = blockIdx.x, xcd = b & 7, j = (b >> 3) & 7, grp = xcd * 4 + (b >> 6), row0 = grp * 64, mi = b >> 3;
    const int hi = lane >> 5, g = lane >> 4, l15 = lane & 15, l31 = lane & 31, q4 = (lane & 15) >> 2, p4 = lane & 3;
    GAS char* ws = (GAS char*)a.ws;
    const __amdgpu_buffer_rsrc_t rs = __builtin_amdgcn_make_buffer_rsrc(a.ws, 0, WS_END, 0x00020000);
#define LD16(off) (*(const LAS v4u*)(lds + (off)))
#define LD8(off) (*(const LAS v2u*)(lds + (off)))
#define LDF4(off) (*(const LAS f32x4*)(lds + (off)))
#define ST16(off, v) (*(LAS v4u*)(lds + (off)) = (v))
#define ST8(off, v) (*(LAS v2u*)(lds + (off)) = (v))
#define STF4(off, v) (*(LAS f32x4*)(lds + (off)) = (v))
#define STF(off, v) (*(LAS float*)(lds + (off)) = (v))
#define LDF(off) (*(const LAS float*)(lds + (off)))
#define TR(off) __builtin_bit_cast(v2u, __builtin_amdgcn_ds_read_tr16_b64_v4i16((LAS s16x4*)(lds + (off))))
#define GL8(p) (*(const GAS v2u*)(p))
#define CLD16(off) __builtin_amdgcn_raw_buffer_load_b128(rs, (off), 0, 16)
#define CST16(off, v) __builtin_amdgcn_raw_buffer_store_b128((v), rs, (off), 0, 16)
#define CST8(off, v) __builtin_amdgcn_raw_buffer_store_b64((v), rs, (off), 0, 16)
#define AG16(d, vo, sb, imm) asm volatile("global_load_dwordx4 %0, %1, %2 offset:%3" : "=v"(d) : "v"(vo), "s"(sb), "n"(imm))
#define AG8(d, vo, sb, imm) asm volatile("global_load_dwordx2 %0, %1, %2 offset:%3" : "=v"(d) : "v"(vo), "s"(sb), "n"(imm))
#define AG2(d, vo, sb, imm) asm volatile("global_load_ushort %0, %1, %2 offset:%3" : "=v"(d) : "v"(vo), "s"(sb), "n"(imm))
#define AC16(d, vo, so, imm) asm volatile("buffer_load_dwordx4 %0, %1, %2, %3 offen offset:%4 sc1" : "=v"(d) : "v"(vo), "s"(rs), "s"(so), "n"(imm))
#define AC8(d, vo, so, imm) asm volatile("buffer_load_dwordx2 %0, %1, %2, %3 offen offset:%4 sc1" : "=v"(d) : "v"(vo), "s"(rs), "s"(so), "n"(imm))
#define DMA16(dst, vo, sb) asm volatile("s_mov_b32 m0, %0\n\ts_nop 0\n\tglobal_load_lds_dwordx4 %1, %2" :: "s"(ldsb + (dst)), "v"(vo), "s"(sb) : "m0", "memory")
#define DMA4(dst, vo, sb) asm volatile("s_mov_b32 m0, %0\n\ts_nop 0\n\tglobal_load_lds_dword %1, %2" :: "s"(ldsb + (dst)), "v"(vo), "s"(sb) : "m0", "memory")
#define DMA16I(sl, il, vo, ig, sb) do { u32 tv_; asm volatile("s_add_u32 m0, %1, %2\n\tv_add_u32 %0, %4, %3\n\tglobal_load_lds_dwordx4 %0, %5" : "=&v"(tv_) : "s"(sl), "n"(il), "v"(vo), "n"(ig), "s"(sb) : "m0", "scc", "memory"); } while (0)
#define WAITV(n, ...) asm volatile("s_waitcnt vmcnt(%c[cnt])" : __VA_ARGS__ : [cnt] "n"(n) : "memory")
#define TIE(...) asm volatile("" : __VA_ARGS__)
#define TS(i) do { if ((i) == 0) { pta = (u32)__builtin_amdgcn_s_memrealtime(); TIE("+s"(pta)); } } while (0)
    u32 pta = 0;
#define TC(i)
    auto tr2 = [&](u32 o0, u32 o1) { v2u x0 = TR(o0), x1 = TR(o1); return (v4u){x0.x, x0.y, x1.x, x1.y}; };
    TS(0); TC(30);
    if (mi >= 28) {
        u64 pc; u32 len; asm volatile("s_getpc_b64 %0\n.Lfb_code_a:\n\ts_mov_b32 %1, .Lfb_code_b-.Lfb_code_a" : "=s"(pc), "=s"(len));
        const u32 vo = min((u32)((mi - 28) * 512 + t) * 64u, len & ~3u);
        DMA4(L_DUMMY, vo, pc);
    }
    u32 Ev; { const u32 z = 0; asm volatile("global_load_dword %0, %1, %2 sc1" : "=v"(Ev) : "v"(z), "s"(a.ws)); }
    const int m3 = mi / 3, mr3 = mi - 3 * m3; const bool crit = mr3 == 0 && mi < 30, defer = mr3 != 0 && mi < 30;
    const int mcol = mr3 == 0 ? xcd * 160 + min(m3, 9) * 16 : 1280 + xcd * 320 + (min(m3, 9) * 2 + mr3 - 1) * 16;
    auto mod_load = [&](int col, v4u (&wm)[3], u32 (&cc)[3], int part) {
        const u32 mr = t >> 1, r2 = min(mr + 512u, 639u), co = col * 2 + (t & 1) * 16;
        if (part <= 0) { AG2(cc[0], mr * 2, a.c, 0); AG2(cc[1], mr * 2, a.c, 512); AG2(cc[2], r2 * 2, a.c, 0); AG16(wm[0], mr * 7680 + co, a.wmod, 0); }
        if (part < 0 || part == 1) AG16(wm[1], (mr + 256) * 7680 + co, a.wmod, 0);
        if (part < 0 || part == 2) AG16(wm[2], r2 * 7680 + co, a.wmod, 0);
    };
    auto mod_partial = [&](v4u (&wm)[3], u32 (&cc)[3]) {
        float acc[8] = {};
#pragma unroll
        for (int i = 0; i < 3; ++i) {
            const float cv = bflo(cc[i]); u32 sl = pk2(cv * __builtin_amdgcn_rcpf(1.f + __builtin_amdgcn_exp2f(cv * -1.4426950408889634f)), 0.f);
            if (i == 2 && t >= 256) sl = 0u;
            const u32 sh = sl << 16;
#pragma unroll
            for (int e = 0; e < 4; ++e) { acc[2 * e] = dot2(wm[i][e], sl, acc[2 * e]); acc[2 * e + 1] = dot2(wm[i][e], sh, acc[2 * e + 1]); }
        }
#pragma unroll
        for (int i = 0; i < 4; ++i) acc[i] = t32(acc[i], acc[i + 4]);
#pragma unroll
        for (int i = 0; i < 2; ++i) { acc[i] = t16(acc[i], acc[i + 2]); acc[i] += dpp<X2>(acc[i]); acc[i] += dpp<R4>(acc[i]); acc[i] += dpp<R8>(acc[i]); }
        if ((lane & 14) == 0) *(LAS f32x2*)(lds + L_MRED + (w * 16 + 8 * (lane & 1) + 4 * hi + 2 * (g & 1)) * 4) = (f32x2){acc[0], acc[1]};
    };
    v4u wm[3], xr[10]; u32 cc[3];
    const int tq = j >> 1, isV = j & 1; float kn2 = 0;
    const u32 l16 = lane * 16;
    if (crit) { mod_load(mcol, wm, cc, -1); __builtin_amdgcn_s_barrier(); }
    { const u32 vo = (row0 + 8 * w) * 1280 + l16, vt = (row0 + 8 * w + 2 * g) * 1280 + 1024 + l15 * 16;
#pragma unroll
      for (int i = 0; i < 8; ++i) AG16(xr[i], vo + (i / 3) * 3840, a.x, (i % 3) * 1280);
      AG16(xr[8], vt, a.x, 0); AG16(xr[9], vt, a.x, 1280); }
    v4u wm2[3]; u32 cc2[3];
    if (defer) mod_load(mcol, wm2, cc2, -1);
    const u32 slice_vo = ((u32)lane / 10u) * 1120 + l16 + j * 160 + w * 7680, slice_sl = ldsb + w * 960;
#define SLICE(W, dst, k) do { SB(); if ((k) < 13) DMA16I(slice_sl, (dst) + (k) * 7680, slice_vo, (k) * 61440, W); \
                              else if (w < 2 || (w == 2 && lane < 40)) DMA16I(slice_sl, (dst) + (k) * 7680, slice_vo, (k) * 61440, W); SB(); } while (0)
    auto mod_publish = [&](int col, u32 tg) {
        if (lane < 8) { float s0 = 0, s1 = 0;
#pragma unroll
            for (int i = 0; i < 8; ++i) { const f32x2 v = *(const LAS f32x2*)(lds + L_MRED + (i * 16 + 2 * lane) * 4); s0 += v.x; s1 += v.y; }
            CST8(WS_MODG + (col / 2 + lane) * 8, ((v2u){pk2(s0, s1), tg})); } };
    TS(23);
    if (crit) { WAITV(10, "+v"(Ev), "+v"(wm[0]), "+v"(wm[1]), "+v"(wm[2]), "+v"(cc[0]), "+v"(cc[1]), "+v"(cc[2])); mod_partial(wm, cc); bar(); if (w == 0) mod_publish(mcol, rfl(Ev) * 1664525u + 1013904223u); }
    TS(1);
    SLICE(a.wq, L_WQ, 0); SLICE(a.wq, L_WQ, 1);
    if (defer) WAITV(8); else WAITV(2);
    TIE("+v"(Ev), "+v"(xr[0]), "+v"(xr[1]), "+v"(xr[2]), "+v"(xr[3]), "+v"(xr[4]), "+v"(xr[5]), "+v"(xr[6]), "+v"(xr[7]), "+v"(xr[8]), "+v"(xr[9]));
    const u32 tag = rfl(Ev) * 1664525u + 1013904223u;
    v4u mg; const u32 mgo = min(t, 319) * 16;
    f32x2 xn[40];
    {
        float s[10], q[10];
        auto row = [&](int i) { s[i] = 0; q[i] = 0;
#pragma unroll
            for (int e = 0; e < 4; ++e) { s[i] = dot2(xr[i][e], ONE2, s[i]); q[i] = dot2(xr[i][e], xr[i][e], q[i]); } };
        row(0); row(1); SLICE(a.wq, L_WQ, 2); row(2); row(3); row(4); SLICE(a.wq, L_WQ, 3); row(5); row(6); row(7); SLICE(a.wq, L_WQ, 4); row(8); row(9);
#pragma unroll
        for (int i = 0; i < 4; ++i) { s[i] = t32(s[i], s[i + 4]); q[i] = t32(q[i], q[i + 4]); }
        SLICE(a.wq, L_WQ, 5);
        float rsd[2], nb[2];
#pragma unroll
        for (int i = 0; i < 2; ++i) { const float ss = sum16(t16(s[i], s[i + 2]) + s[8 + i]), qq = sum16(t16(q[i], q[i + 2]) + q[8 + i]);
            const float m = ss * (1.f / 640.f); rsd[i] = __builtin_amdgcn_rcpf(ln_s(fmaxf(qq * (1.f / 640.f) - m * m, 0.f))); nb[i] = -bfr(m) * rsd[i]; }
        auto nrm = [&](int i) { const float r_ = i < 8 ? rdl(rsd[i & 1], 16 * (i >> 1)) : rsd[i & 1], n_ = i < 8 ? rdl(nb[i & 1], 16 * (i >> 1)) : nb[i & 1];
#pragma unroll
            for (int e = 0; e < 4; ++e) xn[4 * i + e] = (f32x2){bflo(xr[i][e]), bfhi(xr[i][e])} * r_ + n_;
            TIE("+v"(xn[4 * i]), "+v"(xn[4 * i + 1]), "+v"(xn[4 * i + 2]), "+v"(xn[4 * i + 3])); };
        { const char* kp = isV ? a.wv : a.wk; const u32 sl = ldsb + w * 1024, vo = t * 16;
          DMA16I(sl, L_WKV, vo, 0, kp); nrm(0); DMA16I(sl, L_WKV + 8192, vo, 8192, kp); nrm(1); DMA16I(sl, L_WKV + 16384, vo, 16384, kp); if (w == 0) DMA16I(sl, L_WKV + 24576, vo, 24576, kp); }
        nrm(2); nrm(3); SLICE(a.wq, L_WQ, 6); nrm(4); nrm(5); nrm(6); SLICE(a.wq, L_WQ, 7); nrm(7); nrm(8); nrm(9); if (lane < 60) SLICE(a.wq, L_WQ, 8);
    }
    if ((w >> 1) == tq) {
#pragma unroll
        for (int i = 0; i < 10; ++i) { v4u o;
#pragma unroll
            for (int e = 0; e < 4; ++e) o[e] = pk2(xn[4 * i + e].x, xn[4 * i + e].y);
            ST16(i < 8 ? L_XK + (8 * (w & 1) + i) * HS + l16 : L_XK + (8 * (w & 1) + 2 * g + i - 8) * HS + 1024 + l15 * 16, o); } }
    if (defer) { WAITV(12, "+v"(wm2[0]), "+v"(wm2[1]), "+v"(wm2[2]), "+v"(cc2[0]), "+v"(cc2[1]), "+v"(cc2[2])); mod_partial(wm2, cc2); }
    TS(2);
    WAITV(3); bar();
    AC16(mg, mgo, WS_MODG, 0);
    TS(3);
    const int hh = w & 3, th = w >> 2, ks2 = min(w + 16, 19);
    v4u wf[40], kf[3][2]; f32x2 xk[12];
    { const u32 wb = L_WKV + (8 * g + q4) * 40 + p4 * 8;
#pragma unroll
      for (int i = 0; i < 3; ++i) { const u32 o = wb + (i < 2 ? w + 8 * i : ks2) * 1280;
          const v4u xv = LD16(L_XK + l15 * HS + g * 16 + (i < 2 ? w + 8 * i : ks2) * 64);
          kf[i][0] = tr2(o, o + 160); kf[i][1] = tr2(o + 32, o + 192);
#pragma unroll
          for (int e = 0; e < 4; ++e) xk[4 * i + e] = (f32x2){bflo(xv[e]), bfhi(xv[e])};
          TIE("+v"(xk[4 * i]), "+v"(xk[4 * i + 1]), "+v"(xk[4 * i + 2]), "+v"(xk[4 * i + 3])); } }
    const u32 wqb = L_WQ + (8 * hi + q4) * 160 + (20 * hh + 16 * (g & 1) + 4 * p4) * 2; u32 wq2 = wqb + 20 * 2560; TIE("+v"(wq2));
    auto wq_frag = [&](int ks) { const int k = ks < 27 ? ks : ks - 27; const u32 o = k < 20 ? wqb + k * 2560 : wq2 + (k - 20) * 2560; wf[ks] = tr2(o, o + 640); };
#pragma unroll
    for (int ks = 0; ks < NPRE; ++ks) wq_frag(ks);
    WAITV(0, "+v"(mg));
    if (defer && w == 7) mod_publish(mcol, tag);
    TS(26);
    if (w < 5) {
        for (u32 sp = 0; sp < SPIN_CAP; ++sp) { if (__all(mg.y == tag && mg.w == tag)) break; mg = CLD16(WS_MODG + mgo); }
        f32x4 f = {bflo(mg.x), bfhi(mg.x), bflo(mg.z), bfhi(mg.z)};
        if (t >= 160) { const u32 p0 = pk2(1.f + f.x, 1.f + f.y), p1 = pk2(1.f + f.z, 1.f + f.w); f = (f32x4){bflo(p0), bfhi(p0), bflo(p1), bfhi(p1)}; }
        STF4(L_SA + t * 16, f);
    }
    bar();
    TS(4);
    {
        f32x4 acc0 = {}, acc1 = {}, gv[6], sv[6];
#pragma unroll
        for (int i = 0; i < 6; ++i) { const u32 mo = (i < 4 ? w + 8 * (i >> 1) : ks2) * 128 + g * 32 + (i & 1) * 16; gv[i] = LDF4(L_G1 + mo); sv[i] = LDF4(L_SA + mo); }
        SB();
#pragma unroll
        for (int i = 0; i < 3; ++i) { v4u hf;
#pragma unroll
            for (int e = 0; e < 4; ++e) { const f32x4 gg = gv[2 * i + (e >> 1)], ss = sv[2 * i + (e >> 1)]; const f32x2 v = xk[4 * i + e] * (e & 1 ? gg.zw : gg.xy) + (e & 1 ? ss.zw : ss.xy); hf[e] = pk2(v.x, v.y); }
            if (i == 2 && w >= 4) hf = (v4u){};
            if (isV) { acc0 = mfma16(hf, kf[i][0], acc0); acc1 = mfma16(hf, kf[i][1], acc1); } else { acc0 = mfma16(kf[i][0], hf, acc0); acc1 = mfma16(kf[i][1], hf, acc1); } }
        STF4(L_WKV + w * 1280 + l16, acc0);
        if (isV ? l15 < 4 : g == 0) STF4(L_WKV + w * 1280 + 1024 + (isV ? 4 * g + l15 : l15) * 16, acc1);
    }
    const int key0 = row0 + 16 * tq; const u32 cb = WS_KV + (key0 >> 7) * KVC, tile = (key0 >> 5) & 3, kk = (key0 & 31) + l15, vb = cb + 4096 + tile * 1536 + (tq & 1) * 768;
    u32 kvs0 = isV ? vb + (g & 1) * 384 + l15 * 16 + 8 * (g >> 1) : cb + tile * 1024 + (kk + 32 * (g & 1)) * 16 + 8 * (g >> 1);
    u32 kvs1 = isV ? vb + (q4 & 1) * 384 + (16 + 4 * g + p4) * 16 + 8 * (q4 >> 1) : cb + 10240 + tile * 512 + kk * 16, kvr = L_WKV + g * 1280 + 1024 + l15 * 16;
    TIE("+v"(kvs0), "+v"(kvs1), "+v"(kvr));
    bar();
    TS(5);
    if (w == 0) {
        __builtin_amdgcn_s_setprio(3);
        f32x4 r0[6];
#pragma unroll
        for (int k = 0; k < 6; ++k) r0[k] = LDF4(L_WKV + k * 1280 + l16);
        SB();
        r0[0] += r0[1]; r0[2] += r0[3]; r0[1] = LDF4(L_WKV + 6 * 1280 + l16); r0[3] = LDF4(L_WKV + 7 * 1280 + l16); SB();
        r0[0] += r0[4]; r0[2] += r0[5];
        f32x4 s1 = LDF4(kvr), s2 = LDF4(kvr + 5120); SB();
        const f32x4 s0 = (r0[0] + r0[1]) + (r0[2] + r0[3]);
        const v2u p0 = {pk2(s0.x, s0.y), pk2(s0.z, s0.w)};
        CST8(kvs0, p0); SB();
        s1 += s2;
        const float u0 = t32(s1.x, s1.y), u1 = t32(s1.z, s1.w), u2 = t16(u0, u1);
        const auto uy = __builtin_amdgcn_permlane32_swap(__float_as_uint(u2), __float_as_uint(u2), false, false);
        const u32 pa = pk2(__uint_as_float(uy[0]), __uint_as_float(uy[1]));
        const auto pz = __builtin_amdgcn_permlane16_swap(pa, pa, false, false);
        v2u p1 = {pz[0], pz[1]};
        if (isV) {
            if (g) { const u32 c = p4 == 0 ? ONE2 : 0u; p1 = (v2u){c, c}; }
            if (g < 2) CST8(kvs1, p1);
        } else {
            if (g == 0) CST16(kvs1, ((v4u){p1.x, p1.y, ONE2, ONE2}));
            kn2 = dot2(p0.x, p0.x, dot2(p0.y, p0.y, 0.f));
            kn2 = t16(kn2, kn2); kn2 = t32(kn2, kn2);
            kn2 = dot2(p1.x, p1.x, dot2(p1.y, p1.y, kn2));
            kn2 = __uint_as_float(umax16(__float_as_uint(kn2)));
        }
    }
    TS(6);
    {
        const u32 hb0 = L_H + 8 * w * HS + l16, ht0 = L_H + (8 * w + 2 * g) * HS + 1024 + l15 * 16;
        f32x4 gm[2], sm[2], gt[2], st[2];
#pragma unroll
        for (int k = 0; k < 2; ++k) { gm[k] = LDF4(L_G1 + lane * 32 + k * 16); sm[k] = LDF4(L_SA + lane * 32 + k * 16); }
        SLICE(a.wq, L_WQ - 69120, 9); SLICE(a.wq, L_WQ - 69120, 10);
#pragma unroll
        for (int i = 0; i < 10; ++i) { v4u o;
            if (i == 4) {
#pragma unroll
                for (int k = 0; k < 2; ++k) { gt[k] = LDF4(L_G1 + 2048 + l15 * 32 + k * 16); st[k] = LDF4(L_SA + 2048 + l15 * 32 + k * 16); }
                SLICE(a.wq, L_WQ - 69120, 12); SLICE(a.wq, L_WQ - 69120, 13); }
            if (i == 2) SLICE(a.wq, L_WQ - 69120, 11);
#pragma unroll
            for (int e = 0; e < 4; ++e) { const f32x4 gg = i < 8 ? gm[e >> 1] : gt[e >> 1], ss = i < 8 ? sm[e >> 1] : st[e >> 1];
                const f32x2 v = xn[4 * i + e] * (e & 1 ? gg.zw : gg.xy) + (e & 1 ? ss.zw : ss.xy); o[e] = pk2(v.x, v.y); }
            if (i == KVF_I && w == 0) { WAITV(5); if (lane == 0) CST8(WS_KVF + b * 8, ((v2u){__float_as_uint(kn2), tag})); __builtin_amdgcn_s_setprio(0); TS(7); }
            ST16(i >= 8 ? ht0 + (i - 8) * HS : hb0 + i * HS, o); SB(); }
    }
    if (w == 0) WAITV(1); else WAITV(0);
    bar();
    TS(24);
    u32 ppd[3], ppv[3]; v4u ppf[3];
    const bool pp3 = w == 1 && b < 224, pp3w2 = b >= 112;
    auto pp_addr = [&](int r) { const int i = r < 2 ? b * 8 + w : 2048 + min(pp3w2 ? b - 112 : b, 111); const bool isw2 = r < 2 ? r == 1 : pp3w2;
        if (isw2) { const int ks = i / 40, nt = i - ks * 40; ppv[r] = ks * 40960 + nt * 32 + (lane >> 1) * 1280 + (lane & 1) * 16; ppd[r] = WS_W2F + (nt * 54 + ks) * 1024; }
        else { const int ks = i / 54, nt = i - ks * 54, jj = ks / 5, ii = ks - jj * 5, lr = lane >> 2;
            ppv[r] = (80 * jj + (ii < 4 ? 32 * (ii >> 1) + 8 * (ii & 1) : 64)) * 3456 + nt * 64 + (lr + (ii < 4 ? (lr & 8) : 0)) * 3456 + p4 * 16; ppd[r] = WS_W1F + (nt * 40 + ks) * 1024; }
        ppd[r] = rfl(ppd[r]); TIE("+v"(ppv[r]), "+s"(ppd[r])); };
    auto pp_load = [&](int r) { DMA16(L_PP + (r < 2 ? w + 8 * r : 16) * 1024, ppv[r], (r < 2 ? r == 1 : pp3w2) ? a.w2 : a.w1); };
    auto pp_read = [&](int r) { const bool isw2 = r < 2 ? r == 1 : pp3w2;
        const u32 o = L_PP + (r < 2 ? w + 8 * r : 16) * 1024 + (isw2 ? (16 * (g >> 1) + 4 * (g & 1) + q4) * 32 + p4 * 8 : (4 * hi + q4) * 64 + (g & 1) * 32 + p4 * 8);
        ppf[r] = tr2(o, o + (isw2 ? 256 : 512)); };
    auto pp_store = [&](int r) { __builtin_amdgcn_raw_buffer_store_b128(ppf[r], rs, l16, ppd[r], 16); };
    v4u qf0, qf1, st0; v2u st1;
    const u32 kvo0 = t * 16, kvo1 = 8192 + t * 8;
    auto kv_load = [&](int c) { AC16(st0, kvo0, WS_KV + c * KVC, 0); AC8(st1, kvo1, WS_KV + c * KVC, 0); };
    auto kv_write = [&](u32 slot) { ST16(slot + kvo0, st0); ST8(slot + kvo1, st1); };
    {
        f32x16 acc = {}; v4u hq[QD];
        const u32 hb = L_H + (32 * th + l31) * HS + hi * 16;
#pragma unroll
        for (int ks = 0; ks < QD; ++ks) hq[ks] = LD16(hb + ks * 32);
#pragma unroll
        for (int ks = 0; ks < 40; ++ks) {
            SB(); acc = mfma32(wf[ks], hq[ks % QD], acc); SB();
            if (ks + QD < 40) hq[ks % QD] = LD16(hb + (ks + QD) * 32);
            if (ks + WD >= NPRE && ks + WD < 40) wq_frag(ks + WD);
            if (ks == 1 || ks == 3 || ks == 5) pp_addr(ks >> 1);
        }
        SB();
        const float sc = 0.22360679774997896f * 1.4426950408889634f;
        qf0 = (v4u){pk2(acc[0] * sc, acc[1] * sc), pk2(acc[2] * sc, acc[3] * sc), pk2(acc[4] * sc, acc[5] * sc), pk2(acc[6] * sc, acc[7] * sc)};
        qf1 = (v4u){hi ? 0u : pk2(acc[8] * sc, acc[9] * sc), hi ? 0u : pk2(acc[10] * sc, acc[11] * sc), 0u, 0u};
        float qn2 = dot2(qf0.x, qf0.x, dot2(qf0.y, qf0.y, dot2(qf0.z, qf0.z, dot2(qf0.w, qf0.w, dot2(qf1.x, qf1.x, dot2(qf1.y, qf1.y, 0.f))))));
        qn2 = t32(qn2, qn2);
        TS(8);
        v4u kv0[9];
        if (w < 4) {
            v4u f0, f1;
            for (u32 sp = 0; sp < SPIN_CAP; ++sp) { f0 = CLD16(WS_KVF + l16); f1 = CLD16(WS_KVF + 1024 + l16);
                if (__all(f0.y == tag && f0.w == tag && f1.y == tag && f1.w == tag)) break;
                __builtin_amdgcn_s_sleep(PSLEEP); }
#pragma unroll
            for (int i = 0; i < 9; ++i) AC16(kv0[i], kvo0, WS_KV + i * 4096, 0);
            const u32 km = umax64(max(max(f0.x, f0.z), max(f1.x, f1.z)));
            if (t == 0) STF(L_A, __uint_as_float(km));
        }
        TS(9);
        bar();
        if (w < 4) {
            WAITV(5, "+v"(kv0[0]), "+v"(kv0[1]), "+v"(kv0[2]), "+v"(kv0[3]));
#pragma unroll
            for (int i = 0; i < 4; ++i) ST16(L_RING + i * 4096 + kvo0, kv0[i]);
            WAITV(0, "+v"(kv0[4]), "+v"(kv0[5]), "+v"(kv0[6]), "+v"(kv0[7]), "+v"(kv0[8]));
#pragma unroll
            for (int i = 4; i < 9; ++i) ST16(L_RING + i * 4096 + kvo0, kv0[i]);
        }
        const float kmax2 = LDF(L_A);
        const float Msh = bfr(__builtin_amdgcn_sqrtf(qn2 * kmax2) * 1.01f + 1e-30f);
        qf1.z = hi ? 0u : (pk2(-Msh, 0.f) & 0xffffu);
        TS(25);
    }
    bar();
    if (b == 0 && t == 128) __hip_atomic_store((GAS u32*)(ws + WS_E), tag, RLX_AGENT);
    TS(10); TC(28);
    const int ntile = j < 6 ? 7 : 6, nt0 = j < 6 ? 7 * j : 42 + 6 * (j - 6); const bool has_tile = w < ntile;
    const u32 w1o = WS_W1F + (nt0 + min(w, ntile - 1)) * 40960;
    v4u w1f[40];
    f32x16 oacc = {};
    {
        const u32 ak0 = L_RING + lane * 16, ak1 = L_RING + 10240 + l31 * 16, av = L_RING + 4096 + (hi * 24 + (l31 < 24 ? l31 : (l31 < 28 ? l31 - 4 : l31 - 12))) * 16;
        v4u k0 = LD16(ak0), k1 = LD16(ak1);
        f32x16 sc = mfma32(k0, qf0, (f32x16){}); sc = mfma32(k1, qf1, sc);
        k0 = LD16(ak0 + 1024); k1 = LD16(ak1 + 512);
        v4u p1 = {}, v1 = {}, pf0, pf1;
#define EX(i) __builtin_amdgcn_exp2f(sc[i])
        const int mu = t - 320; const u32 mgo2 = (t < 320 ? 1920 + 4 * t : (mu < 20 ? 1280 + 80 * j + 4 * mu : 3200 + 80 * j + 4 * (min(mu, 39) - 20))) * 4;
        auto chunk = [&](auto CC) {
            constexpr int c = decltype(CC)::value; constexpr u32 cur = (c % 3) * KVC, nxt = ((c + 1) % 3) * KVC, fre = ((c + 2) % 3) * KVC;
            constexpr int pc = c - 1, NY = pc < 6 ? 2 : 5;
#pragma unroll
            for (int i = 0; i < 4; ++i) {
                v4u p0; f32x16 sn; float e[16];
                const u32 kb = i < 2 ? cur + (i + 2) * 1024 : nxt + (i - 2) * 1024, kb1 = i < 2 ? cur + (i + 2) * 512 : nxt + (i - 2) * 512;
                sn = mfma32(k0, qf0, (f32x16){}); SB();
                const v4u v0 = LD16(cur + av + i * 1536); k0 = LD16(kb + ak0); SB();
                e[0] = EX(0); e[1] = EX(1); e[2] = EX(2); e[3] = EX(3); SB();
                oacc = mfma32(v1, p1, oacc); SB();
                v1 = LD16(cur + av + i * 1536 + 768); SB();
                e[4] = EX(4); e[5] = EX(5); SB(); p0.x = pk2(e[0], e[1]); p0.y = pk2(e[2], e[3]); SB(); e[6] = EX(6); SB();
                sn = mfma32(k1, qf1, sn); SB();
                k1 = LD16(kb1 + ak1); SB();
                e[7] = EX(7); e[8] = EX(8); SB(); p0.z = pk2(e[4], e[5]); SB(); e[9] = EX(9); SB(); p0.w = pk2(e[6], e[7]); SB(); e[10] = EX(10); e[11] = EX(11); SB();
                oacc = mfma32(v0, p0, oacc); SB();
                e[12] = EX(12); e[13] = EX(13); SB(); p1.x = pk2(e[8], e[9]); SB(); e[14] = EX(14); e[15] = EX(15); SB(); p1.y = pk2(e[10], e[11]); p1.z = pk2(e[12], e[13]); p1.w = pk2(e[14], e[15]);
                TIE("+v"(p1));
                if (i == 0) {
                    if constexpr (c > 0 && c < 14) { WAITV(NY, "+v"(st0), "+v"(st1)); kv_write(L_RING + fre); }
                    if constexpr (c == 2) { pp_read(0); pp_read(1); pp_read(2); }
                    if constexpr (c == 4) { if (t == 64) CST8(WS_WF + b * 8, ((v2u){0u, tag})); }
                    if constexpr (c == 5) { if (w == 7) { AC16(pf0, l16, WS_WF, 0); AC16(pf1, l16, WS_WF, 1024); } }
                }
                if (i == 1) {
                    if constexpr (c == 2) { pp_store(0); pp_store(1); if (pp3) pp_store(2); }
                    if constexpr (c == 14) AC16(mg, mgo2, WS_MODG, 0);
                    if constexpr (c < 13) kv_load(c + 3);
                }
                if (i == 3) { if constexpr (c == 5) { if (w == 7) {
                    WAITV(3, "+v"(pf0), "+v"(pf1));
                    for (u32 sp = 0; sp < SPIN_CAP; ++sp) { if (__all(pf0.y == tag && pf0.w == tag && pf1.y == tag && pf1.w == tag)) break;
                        __builtin_amdgcn_s_sleep(4); pf0 = CLD16(WS_WF + l16); pf1 = CLD16(WS_WF + 1024 + l16); } } } }
                if (i >= 2) {
                    if constexpr (c == 0) { pp_load(i - 2); if (i == 3 && pp3) pp_load(2); }
                    if constexpr (c >= 1 && c < 6) { if (i == 2) SLICE(a.wo, L_WO, 2 * c - 2); else SLICE(a.wo, L_WO, 2 * c - 1); }
                    if constexpr (c >= 6 && c < 14) { const int k = 4 * (c - 6) + 2 * (i - 2); AC16(w1f[k], l16, w1o + (k & ~3) * 1024, (k & 3) * 1024); AC16(w1f[k + 1], l16, w1o + (k & ~3) * 1024, ((k + 1) & 3) * 1024); }
                    if constexpr (c >= 6 && c < 10) { if (i == 2) SLICE(a.wo, L_WO, c + 4); }
                    if constexpr (c >= 9) { if (i == 3 && lane < TOUCH_N) DMA4(L_DUMMY, WS_W2F + min((u32)(((mi * 7 + c - 9) * 8 + w) * TOUCH_N + lane), 2160u * 1024u / TOUCH_S - 1) * TOUCH_S, a.ws); }
                }
                SB();
                sc = sn;
            }
            bar();
        };
        chunk(IC<0>{}); chunk(IC<1>{}); chunk(IC<2>{}); chunk(IC<3>{}); chunk(IC<4>{}); chunk(IC<5>{}); chunk(IC<6>{}); chunk(IC<7>{});
        chunk(IC<8>{}); chunk(IC<9>{}); chunk(IC<10>{}); chunk(IC<11>{}); chunk(IC<12>{}); chunk(IC<13>{}); chunk(IC<14>{}); chunk(IC<15>{});
        oacc = mfma32(v1, p1, oacc);
    }
    TS(11); TC(29);
    {
    int t2 = threadIdx.x; TIE("+v"(t2));
    const int t = t2, lane = t & 63, hi = lane >> 5, g = lane >> 4, l15 = lane & 15, l31 = lane & 31, q4 = (lane & 15) >> 2, p4 = lane & 3; const u32 l16 = lane * 16;
    float lsum = hi32(oacc[8]);
    if (__any(!(lsum >= 1e-12f && lsum < 3e38f))) {
        qf1.z = 0u;
        const u32 ak0 = WS_KV + lane * 16, ak1 = WS_KV + 10240 + l31 * 16, av = WS_KV + 4096 + (hi * 24 + (l31 < 24 ? l31 : 20)) * 16;
        float mx = -__builtin_inff();
        for (int tI = 0; tI < 64; ++tI) { const u32 cb = (tI >> 2) * KVC, i = tI & 3;
            const v4u k0 = CLD16(cb + ak0 + i * 1024), k1 = CLD16(cb + ak1 + i * 512);
            f32x16 s = mfma32(k0, qf0, (f32x16){}); s = mfma32(k1, qf1, s);
#pragma unroll
            for (int r = 0; r < 16; ++r) mx = fmaxf(mx, s[r]); }
        mx = fmaxf(mx, __shfl_xor(mx, 32));
        oacc = (f32x16){};
        for (int tI = 0; tI < 64; ++tI) { const u32 cb = (tI >> 2) * KVC, i = tI & 3;
            const v4u k0 = CLD16(cb + ak0 + i * 1024), k1 = CLD16(cb + ak1 + i * 512);
            const v4u v0 = CLD16(cb + av + i * 1536), v1 = CLD16(cb + av + i * 1536 + 768);
            f32x16 s = mfma32(k0, qf0, (f32x16){}); s = mfma32(k1, qf1, s);
            v4u p0, p1;
#pragma unroll
            for (int e = 0; e < 4; ++e) { p0[e] = pk2(__builtin_amdgcn_exp2f(s[2 * e] - mx), __builtin_amdgcn_exp2f(s[2 * e + 1] - mx));
                                          p1[e] = pk2(__builtin_amdgcn_exp2f(s[8 + 2 * e] - mx), __builtin_amdgcn_exp2f(s[9 + 2 * e] - mx)); }
            oacc = mfma32(v0, p0, oacc); oacc = mfma32(v1, p1, oacc); }
        lsum = hi32(oacc[8]);
    }
    const u32 gfl = WS_GF + grp * 1024;
    const int mp = w >> 1, par = w & 1;
    v2u xres[3]; u32 b1v;
    {
        const float il = __builtin_amdgcn_rcpf(lsum); const int mt = 2 * th + (l31 >> 4);
        const v4u o0 = {pk2(oacc[0] * il, oacc[1] * il), pk2(oacc[2] * il, oacc[3] * il), pk2(oacc[4] * il, oacc[5] * il), pk2(oacc[6] * il, oacc[7] * il)};
        const u32 ob = WS_O + grp * 81920 + mt * 20480 + l15 * 16;
        CST16(ob + (2 * j + (hh >> 1)) * 1024 + (2 * (hh & 1) + hi) * 256, o0);
        const int ci = 64 + 2 * j + (hh >> 1);
        if (!hi) CST8(ob + ci * 256 + 8 * (hh & 1), ((v2u){pk2(oacc[8] * il, oacc[9] * il), pk2(oacc[10] * il, oacc[11] * il)}));
        SB();
        const u32 xo = (row0 + 16 * mp + l15) * 1280 + j * 160 + g * 8;
        AG8(xres[0], xo + par * 64, a.x, 0); AG8(xres[1], xo + par * 64, a.x, 32); AG8(xres[2], xo, a.x, 128);
        AG2(b1v, ((nt0 + min(w, ntile - 1)) * 32 + l31) * 2, a.b1, 0);
    }
    {
        WAITV(6, "+v"(mg));
        if (t == 511) STF(L_MRED, 0.f);
        const int mu = t - 320; const u32 mgo2 = (t < 320 ? 1920 + 4 * t : (mu < 20 ? 1280 + 80 * j + 4 * mu : 3200 + 80 * j + 4 * (min(mu, 39) - 20))) * 4;
                        if (w < 6) {
                            for (u32 sp = 0; sp < SPIN_CAP; ++sp) { if (__all(mg.y == tag && mg.w == tag)) break; mg = CLD16(WS_MODG + mgo2); }
                            v2u v = {mg.x, mg.z}; f32x4 f = {bflo(v.x), bfhi(v.x), bflo(v.y), bfhi(v.y)};
                            if (w < 5) {
                                const int isg = t >= 160, tp = t - 160 * isg, jj = (tp * 205) >> 12, r4 = tp - jj * 20;
                                if (isg) { v = (v2u){pk2(1.f + f.x, 1.f + f.y), pk2(1.f + f.z, 1.f + f.w)}; f = (f32x4){bflo(v.x), bfhi(v.x), bflo(v.y), bfhi(v.y)}; }
                                const int o = r4 < 16 ? (r4 >> 3) * 64 + (r4 & 3) * 16 + (r4 & 4) * 2 : 128 + (r4 & 1) * 16 + (r4 & 2) * 4;
                                ST8(L_AUX + (isg ? 0 : 1280) + jj * 160 + o, v);
                                if (jj == j) { if (isg) STF4(L_OWN + 320 + r4 * 16, f); ST8(L_OWN + (isg ? 960 : 1120) + r4 * 8, v); }
                            } else if (mu < 40) STF4(L_OWN + (mu < 20 ? mu * 16 : 640 + (mu - 20) * 16), f);
                        } else if (t < 464) ST16(L_AUX + 2560 + (t - 384) * 16, ((v4u){ONE2, ONE2, ONE2, ONE2}));
    }
#define W1T(k) "+v"(w1f[k]), "+v"(w1f[k + 1]), "+v"(w1f[k + 2]), "+v"(w1f[k + 3]), "+v"(w1f[k + 4]), "+v"(w1f[k + 5]), "+v"(w1f[k + 6]), "+v"(w1f[k + 7])
    WAITV(4, W1T(0), W1T(8)); TIE(W1T(16), W1T(24));
    if (lane < 12) DMA4(L_DUMMY, WS_O + grp * 81920 + (2 * th + (lane & 1)) * 20480 + (lane < 8 ? (2 * j + (hh >> 1)) * 1024 + (hh & 1) * 512 + (lane >> 1) * 128 : (64 + 2 * j + (hh >> 1)) * 256 + (lane & 2) * 64), a.ws);
    bar();
    if (t == 0) __hip_atomic_store((GAS u32*)(ws + gfl + j * 128), tag, RLX_AGENT);
    TS(12);
    auto wait_flags = [&](int phase, int first, int n) {
        for (u32 sp = 0; sp < SPIN_CAP; ++sp) { const u32 f = __builtin_amdgcn_raw_buffer_load_b32(rs, gfl + phase * 32768 + (first + (lane & (n - 1))) * 128, 0, 16);
            if (__all(f == tag)) break; __builtin_amdgcn_s_sleep(1); } };
    const u32 ab = L_AUX + min(l31, 2) * 1280 + hi * 16;
    float up[3];
    auto ua_part = [&](auto K0, auto K1) { constexpr int k0 = decltype(K0)::value, k1 = decltype(K1)::value; f32x16 ua = {}; v4u af[4];
#pragma unroll
        for (int ks = k0; ks < k0 + 4; ++ks) af[ks & 3] = LD16(ab + ks * 32);
#pragma unroll
        for (int ks = k0; ks < k1; ++ks) { SB(); ua = mfma32(af[ks & 3], w1f[ks], ua); SB(); if (ks + 4 < k1) af[ks & 3] = LD16(ab + (ks + 4) * 32); }
#pragma unroll
        for (int i = 0; i < 3; ++i) up[i] = k0 ? up[i] + ua[i] : ua[i];
        TIE("+v"(up[0]), "+v"(up[1]), "+v"(up[2])); };
    ua_part(IC<0>{}, IC<UA1>{});
    u32 x1p[6];
    {
        v4u xf[10], wr[WOD]; f32x4 acc[5] = {};
        const u32 wm_ = L_WO + (320 * par + 20 * (g >> 1) + 4 * (g & 1) + q4) * 160 + p4 * 8, wl_ = L_WO + (320 * par + 40 * g + 16 + q4) * 160 + p4 * 8;
        auto wo_frag = [&](int s_) { const int i = s_ / 5, nt = s_ % 5; const u32 o = i < 8 ? wm_ + i * 6400 + nt * 32 : wl_ + (i - 8) * 25600 + nt * 32; wr[s_ % WOD] = tr2(o, o + (i < 8 ? 1280 : 3200)); };
#pragma unroll
        for (int s_ = 0; s_ < WOD; ++s_) wo_frag(s_);
        SB();
        wait_flags(0, 4 * par, 4);
        TS(13);
        { const u32 ob = WS_O + grp * 81920 + mp * 20480 + par * 8192, ot = WS_O + grp * 81920 + mp * 20480 + 16384 + par * 2048;
#pragma unroll
          for (int i = 0; i < 10; ++i) { if (i < 8) AC16(xf[i], l16, ob + (i & 4) * 1024, (i & 3) * 1024); else AC16(xf[i], l16, ot, (i - 8) * 1024); } }
#pragma unroll
        for (int s_ = 0; s_ < 50; ++s_) { const int i = s_ / 5, nt = s_ % 5;
            if (nt == 0) WAITV(9 - i, "+v"(xf[i]));
            SB(); acc[nt] = mfma16(wr[s_ % WOD], xf[i], acc[nt]); SB();
            if (s_ + WOD < 50) wo_frag(s_ + WOD); }
        SB(); TIE("+v"(xres[0]), "+v"(xres[1]), "+v"(xres[2]), "+v"(b1v)); SB();
        const u32 rb = L_WRED + w * 3072 + l16;
        STF4(rb, par ? acc[0] : acc[2]); STF4(rb + 1024, par ? acc[1] : acc[3]); STF4(rb + 2048, acc[4]);
        f32x4 ga[3], g2[3]; v2u g2p[3], sbp[3];
#pragma unroll
        for (int i = 0; i < 3; ++i) { const u32 co = (i < 2 ? par * 128 + i * 64 : 256) + g * 16;
            ga[i] = LDF4(L_OWN + co); g2[i] = LDF4(L_OWN + 320 + co); g2p[i] = LD8(L_OWN + 960 + co / 2); sbp[i] = LD8(L_OWN + 1120 + co / 2); }
        u32 xb = WS_XG + grp * 81920 + (mp >> 1) * 40960 + (16 * (mp & 1) + l15 + 32 * (g & 1)) * 16 + 5 * j * 1024, xb0 = xb + (2 * par + (g >> 1)) * 1024, xb1 = xb + 4096 + 8 * (g >> 1);
        u32 sto = WS_ST + (grp * 8 + j) * 4096 + (par * 3 + g) * 512 + (16 * mp + l15) * 8;
        f32x4 r[3]; r[0] = par ? acc[2] : acc[0]; r[1] = par ? acc[3] : acc[1]; r[2] = acc[4];
        TIE("+v"(xb0), "+v"(xb1), "+v"(sto), "+v"(r[0]), "+v"(r[1]));
        bar();
        TS(22);
        const u32 pb = L_WRED + (w ^ 1) * 3072 + l16;
        r[0] += LDF4(pb); r[1] += LDF4(pb + 1024); r[2] += LDF4(pb + 2048);
        u32 xg[6];
#pragma unroll
        for (int i = 0; i < 3; ++i) {
            x1p[2 * i] = pk2(r[i][0] * ga[i][0] + bflo(xres[i].x), r[i][1] * ga[i][1] + bfhi(xres[i].x)); x1p[2 * i + 1] = pk2(r[i][2] * ga[i][2] + bflo(xres[i].y), r[i][3] * ga[i][3] + bfhi(xres[i].y));
            xg[2 * i] = pk2(bflo(x1p[2 * i]) * g2[i][0], bfhi(x1p[2 * i]) * g2[i][1]); xg[2 * i + 1] = pk2(bflo(x1p[2 * i + 1]) * g2[i][2], bfhi(x1p[2 * i + 1]) * g2[i][3]);
        }
        CST16(xb0, ((v4u){xg[0], xg[1], xg[2], xg[3]}));
        if (hi == par) CST8(xb1, ((v2u){xg[4], xg[5]}));
        float sm[6] = {};
#pragma unroll
        for (int i = 0; i < 6; ++i) { const bool off = i >= 4 && hi != par; const u32 xa = off ? 0u : x1p[i], ya = off ? 0u : xg[i];
            sm[0] = dot2(xa, ONE2, sm[0]); sm[1] = dot2(xa, xa, sm[1]); sm[2] = dot2(ya, ONE2, sm[2]); sm[3] = dot2(ya, ya, sm[3]);
            sm[4] = dot2(ya, g2p[i >> 1][i & 1], sm[4]); sm[5] = dot2(ya, sbp[i >> 1][i & 1], sm[5]); }
        const float e0 = t32(sm[0], sm[4]), e1 = t32(sm[1], sm[5]), e2 = t32(sm[2], sm[2]), e3 = t32(sm[3], sm[3]);
        const float f0 = t16(e0, e2), f1 = t16(e1, e3);
        if (g < 3) CST8(sto, ((v2u){__float_as_uint(f0), __float_as_uint(f1)}));
        SB();
#pragma unroll
        for (int k = 32; k < 40; ++k) AC16(w1f[k], l16, w1o + (k & ~3) * 1024, (k & 3) * 1024);
        WAITV(8);
        if ((lane & 7) == 0) DMA4(L_DUMMY, xb0, a.ws);
        if ((lane & 7) == 0) DMA4(L_DUMMY, (lane & 8) && g < 3 ? sto : (xb1 & ~255u) + hi * 128, a.ws);
    }
    bar();
    if (t == 0) __hip_atomic_store((GAS u32*)(ws + gfl + 32768 + j * 128), tag, RLX_AGENT);
    TS(14);
    v4u wex;
    {
        ua_part(IC<UA1>{}, IC<32>{});
        WAITV(2 * TOUCH_OWN, W1T(32));
        ua_part(IC<32>{}, IC<40>{});
        {
            const u32 h0 = pk2(up[0], up[1]), h2 = pk2(up[2], 0.f);
            const u32 l0 = pk2(up[0] - bflo(h0), up[1] - bfhi(h0)), l2 = pk2(up[2] - bflo(h2), 0.f);
            wex.x = hi ? (b1v | (b1v << 16)) : ((h0 & 0xffffu) | (l0 << 16)); wex.y = hi ? 0u : ((h0 >> 16) | (l0 & 0xffff0000u));
            wex.z = hi ? 0u : ((h2 & 0xffffu) | (l2 << 16)); wex.w = hi ? 0u : (h2 & 0xffffu);
            TIE("+v"(wex));
        }
    }
    float cst[5];
    if (w == 7) {
#pragma unroll
        for (int i = 0; i < 5; ++i) cst[i] = 0;
#pragma unroll
        for (int r = 0; r < 2; ++r) { const int ch = lane + 64 * r; v4u gv = LD16(L_AUX + min(ch, 79) * 16), sv = LD16(L_AUX + 1280 + min(ch, 79) * 16);
#pragma unroll
            for (int e = 0; e < 4; ++e) { const u32 g_ = ch < 80 ? gv[e] : 0u, s_ = ch < 80 ? sv[e] : 0u;
                cst[0] = dot2(g_, ONE2, cst[0]); cst[1] = dot2(g_, g_, cst[1]); cst[2] = dot2(g_, s_, cst[2]); cst[3] = dot2(s_, ONE2, cst[3]); cst[4] = dot2(s_, s_, cst[4]); } }
#pragma unroll
        for (int i = 0; i < 5; ++i) { cst[i] = sum16(cst[i]); cst[i] = t16(cst[i], cst[i]); cst[i] = t32(cst[i], cst[i]); }
    }
    wait_flags(1, w, 1);
    TS(15);
    {
        v4u xi[13]; const u32 xb = WS_XG + grp * 81920 + 5 * w * 1024, sbo = WS_ST + (grp * 8 + w) * 4096;
#pragma unroll
        for (int i = 0; i < 3; ++i) AC16(xi[10 + i], l16, sbo, i * 1024);
#pragma unroll
        for (int i = 0; i < 10; ++i) AC16(xi[i], l16, xb + (i / 5) * 40960 + (i % 5 == 4 ? 4096 : 0), (i % 5 & 3) * 1024);
        WAITV(10, "+v"(xi[10]), "+v"(xi[11]), "+v"(xi[12]));
#pragma unroll
        for (int i = 0; i < 3; ++i) ST16(L_STP + w * 3072 + i * 1024 + l16, xi[10 + i]);
#pragma unroll
        for (int i = 0; i < 10; ++i) { WAITV(9 - i, "+v"(xi[i])); ST16(L_X + (i / 5) * 40960 + (5 * w + i % 5) * 1024 + l16, xi[i]); }
    }
    bar();
    TS(16);
    const int ks0 = w < 6 ? 7 * w : 42 + 6 * (w - 6), nks = w < 6 ? 7 : 6;
    v2u b2v[3];
#define AC16R(d, vo, so, imm) asm volatile("buffer_load_dwordx4 %0, %1, %2, %3 offen offset:%4 sc1" : "+v"(d) : "v"(vo), "s"(rs), "s"(so), "n"(imm))
    {
        const u32 w2o = WS_W2F + (5 * j * 54 + ks0) * 1024, w2l = WS_W2F + (5 * j * 54 + ks0 + nks - 1) * 1024;
        auto w2_load = [&](int k) { const int i = k / 5, nt = k % 5; if (i < 6) AC16R(w1f[k], l16, w2o + nt * 55296 + (i & ~3) * 1024, (i & 3) * 1024); else AC16R(w1f[k], l16, w2l + nt * 55296, 0); };
        f32x16 a0 = {}, a1 = {};
        if (w == 7) {
            __builtin_amdgcn_s_setprio(3);
            f32x2 us[3] = {};
#pragma unroll
            for (int i = 0; i < 48; ++i) us[i % 3] += *(const LAS f32x2*)(lds + L_STP + i * 512 + lane * 8);
            const f32x4 u0 = {us[0].x, us[0].y, us[1].x, us[1].y}; const f32x2 u1 = us[2];
            const float in = 1.f / 640.f, m1 = u0.x * in, m1b = bfr(m1), s1 = ln_s(fmaxf(u0.y * in - m1 * m1, 0.f)), r1 = 1.f / s1;
            const float m2 = (r1 * (u0.z - m1b * cst[0]) + cst[3]) * in;
            const float q2 = (r1 * r1 * (u0.w - 2.f * m1b * u1.x + m1b * m1b * cst[1]) + 2.f * r1 * (u1.y - m1b * cst[2]) + cst[4]) * in;
            const float s2 = ln_s(fmaxf(q2 - m2 * m2, 0.f)), bw = -bfr(m2) * s1, gm = s1 * s2;
            const u32 bh = pk2(bw, gm), bl = pk2(bw - bflo(bh), gm - bfhi(bh));
            const u32 xe = L_XE + hi * 1024 + l31 * 16;
            ST16(xe, ((v4u){pk2(-m1b, -m1b), pk2(s1, s1), (bh & 0xffffu) | (bh << 16), bl & 0xffffu}));
            ST16(xe + 512, ((v4u){(bh >> 16) | (bl & 0xffff0000u), 0u, 0u, 0u}));
            STF(L_A + lane * 4, 1.f / gm);
            asm volatile("" ::: "memory"); STF(L_MRED, 1.f);
            __builtin_amdgcn_s_setprio(0);
        }
        {
            u32 xa = L_X + l16, xb2 = L_X + 40960 + l16; TIE("+v"(xb2));
            v4u x0[3], x1[3];
#pragma unroll
            for (int ks = 0; ks < 2; ++ks) { x0[ks] = LD16(xa + ks * 1024); x1[ks] = LD16(xb2 + ks * 1024); }
#pragma unroll
            for (int ks = 0; ks < 40; ++ks) {
                if (ks + 2 < 40) { x0[(ks + 2) % 3] = LD16(xa + (ks + 2) * 1024); x1[(ks + 2) % 3] = LD16(xb2 + (ks + 2) * 1024); }
                SB();
                if (has_tile) { a0 = mfma32(w1f[ks], x0[ks % 3], a0); a1 = mfma32(w1f[ks], x1[ks % 3], a1); }
                SB();
                if (ks >= 1 && ks < 36) w2_load(ks - 1); }
        }
        { const u32 bo = (80 * j + par * 32 + g * 4) * 2; AG8(b2v[0], bo, a.b2, 0); AG8(b2v[1], bo, a.b2, 32); AG8(b2v[2], (80 * j + 64 + g * 4) * 2, a.b2, 0); }
        TS(17);
        u32 hb = WS_H + grp * 221184 + ((l31 >> 4) * 54 + nt0 + min(w, ntile - 1)) * 1024 + (l15 + 16 * hi) * 16;
        if (has_tile) {
            for (u32 sp = 0; sp < SPIN_CAP; ++sp) { if (*(volatile LAS float*)(lds + L_MRED) == 1.f) break; __builtin_amdgcn_s_sleep(1); }
            asm volatile("" ::: "memory");
            a0 = mfma32(wex, LD16(L_XE + l16), a0); a1 = mfma32(wex, LD16(L_XE + 1024 + l16), a1);
            const float A0 = LDF(L_A + l31 * 4), A1 = LDF(L_A + 128 + l31 * 4);
            const float k1 = -2.f * 0.7978845608028654f * 1.4426950408889634f, k3 = k1 * 0.044715f;
            auto gelu = [&](float z) { return z * __builtin_amdgcn_rcpf(1.f + __builtin_amdgcn_exp2f(z * (k1 + k3 * z * z))); };
#pragma unroll
            for (int tt = 0; tt < 2; ++tt)
#pragma unroll
                for (int hf = 0; hf < 2; ++hf) { v4u o; const float A = tt ? A1 : A0;
#pragma unroll
                    for (int e = 0; e < 4; ++e) { const int r = 8 * hf + 2 * e; o[e] = pk2(gelu(A * (tt ? a1[r] : a0[r])), gelu(A * (tt ? a1[r + 1] : a0[r + 1]))); }
                    CST16(hb + tt * 108 * 1024 + hf * 512, o); }
        }
        WAITV(0, W1T(0), W1T(8), "+v"(b2v[0]), "+v"(b2v[1]), "+v"(b2v[2])); TIE(W1T(16), W1T(24), "+v"(w1f[32]), "+v"(w1f[33]), "+v"(w1f[34]));
        if (has_tile && (lane & 7) < 4) DMA4(L_DUMMY, (hb & ~127u) + (lane & 1) * 512 + (lane & 2) * 54 * 1024, a.ws);
    }
    bar();
    if (t == 0) __hip_atomic_store((GAS u32*)(ws + gfl + 65536 + j * 128), tag, RLX_AGENT);
    TS(18);
    wait_flags(2, w, 1);
    TS(19);
    {
        f32x4 own[3]; v4u hf[14];
        const u32 hb = WS_H + grp * 221184 + ks0 * 1024;
        auto h_load = [&](int m, int i) { const u32 o = hb + ((mp + 1 + m) & 3) * 55296; v4u& d = hf[(m & 1) * 7 + i];
            if (i < 6) AC16(d, l16, o + (i & 4) * 1024, (i & 3) * 1024); else AC16(d, l16, o + (nks - 1) * 1024, 0); };
#pragma unroll
        for (int k = 0; k < 14; ++k) h_load(k / 7, k % 7);
#pragma unroll
        for (int m = 0; m < 4; ++m) { const int mt = (mp + 1 + m) & 3;
            f32x4 acc[5] = {};
#pragma unroll
            for (int i = 0; i < 7; ++i) { v4u& d = hf[(m & 1) * 7 + i];
                WAITV(m < 2 ? 13 : 27 - 7 * m - i, "+v"(d));
                if (i < 6 || nks == 7) {
#pragma unroll
                    for (int nt = 0; nt < 5; ++nt) acc[nt] = mfma16(w1f[i * 5 + nt], d, acc[nt]); }
                SB();
                if (m < 2) h_load(m + 2, i); }
            if (m == 3) {
                const u32 pb = L_PART + mt * 35840 + 6 * 5120 + l16;
                STF4(pb + (par ? 0 : 2048), par ? acc[0] : acc[2]); STF4(pb + (par ? 1024 : 3072), par ? acc[1] : acc[3]);
                if (hi != par) STF4(pb + 4096, acc[4]);
                own[0] = par ? acc[2] : acc[0]; own[1] = par ? acc[3] : acc[1]; own[2] = acc[4];
            } else {
                const u32 pb = L_PART + mt * 35840 + ((w - 2 * mt - 2) & 7) * 5120 + l16;
#pragma unroll
                for (int nt = 0; nt < 5; ++nt) STF4(pb + nt * 1024, acc[nt]);
            }
        }
        f32x4 gb[3], pr[3][7];
#pragma unroll
        for (int i = 0; i < 3; ++i) gb[i] = LDF4(L_OWN + 640 + ((i < 2 ? par * 32 + i * 16 : 64) + g * 4) * 4);
        bar();
        TS(20);
#pragma unroll
        for (int i = 0; i < 3; ++i)
#pragma unroll
            for (int k = 0; k < 7; ++k) pr[i][k] = LDF4(L_PART + mp * 35840 + (i < 2 ? 2 * par + i : 4) * 1024 + l16 + k * 5120);
        SB();
        const __amdgpu_buffer_rsrc_t ro = __builtin_amdgcn_make_buffer_rsrc(a.out, 0, 2048 * 1280, 0x00020000);
        const u32 op = (u32)(row0 + 16 * mp + l15) * 1280 + j * 160 + g * 8;
#pragma unroll
        for (int i = 0; i < 3; ++i) {
            const f32x4 s = ((own[i] + pr[i][0]) + (pr[i][1] + pr[i][2])) + ((pr[i][3] + pr[i][4]) + (pr[i][5] + pr[i][6])); const v2u bb = b2v[i];
            const float o0 = bflo(x1p[2 * i]) + (s.x + bflo(bb.x)) * gb[i].x, o1 = bfhi(x1p[2 * i]) + (s.y + bfhi(bb.x)) * gb[i].y;
            const float o2 = bflo(x1p[2 * i + 1]) + (s.z + bflo(bb.y)) * gb[i].z, o3 = bfhi(x1p[2 * i + 1]) + (s.w + bfhi(bb.y)) * gb[i].w;
            if (i < 2 || hi == par) __builtin_amdgcn_raw_buffer_store_b64((v2u){pk2(o0, o1), pk2(o2, o3)}, ro, op + (i < 2 ? par * 64 + i * 32 : 128), 0, OAUX);
        }
    }
    }
    TS(21); TC(31);
    { while ((u32)__builtin_amdgcn_s_memrealtime() - pta < 10000u) __builtin_amdgcn_s_sleep(1); }
    asm volatile(".Lfb_code_b:");
}

extern "C" void kernel_launch(void* const* d_in, const int* in_sizes, int n_in, void* d_out, int out_size, void* d_ws, size_t ws_size, hipStream_t stream) {
    Args a;
    a.x = (const char*)d_in[0]; a.wq = (const char*)d_in[1]; a.wk = (const char*)d_in[2]; a.wv = (const char*)d_in[3]; a.wo = (const char*)d_in[4];
    a.w1 = (const char*)d_in[6]; a.b1 = (const char*)d_in[7]; a.w2 = (const char*)d_in[8]; a.b2 = (const char*)d_in[9]; a.c = (const char*)d_in[10]; a.wmod = (const char*)d_in[11];
    a.out = (char*)d_out; a.ws = (char*)d_ws;
    (void)hipFuncSetAttribute((const void*)fused_block, hipFuncAttributeMaxDynamicSharedMemorySize, L_TOTAL);
    hipLaunchKernelGGL(fused_block, dim3(256), dim3(512), L_TOTAL, stream, a);
}
```

```cpp
#include <hip/hip_runtime.h>
#include <hip/hip_bf16.h>
typedef unsigned u32; typedef unsigned long long u64; typedef unsigned short u16;
typedef u32 v4u __attribute__((ext_vector_type(4)));
typedef u32 v2u __attribute__((ext_vector_type(2)));
typedef float f32x2 __attribute__((ext_vector_type(2)));
typedef float f32x4 __attribute__((ext_vector_type(4)));
typedef float f32x16 __attribute__((ext_vector_type(16)));
typedef __bf16 bf16x8 __attribute__((ext_vector_type(8)));
typedef __bf16 bf16x2 __attribute__((ext_vector_type(2)));
typedef short s16x4 __attribute__((ext_vector_type(4)));
#define GAS __attribute__((address_space(1)))
#define LAS __attribute__((address_space(3)))
#define RLX_AGENT __ATOMIC_RELAXED, __HIP_MEMORY_SCOPE_AGENT
#define DI __device__ __forceinline__
#define SB() __builtin_amdgcn_sched_barrier(0)

constexpr u32 KVC = 12288;
constexpr u32 WS_E = 0, WS_MODG = 1024, WS_KVF = 16384, WS_WF = 18432, WS_GF = 32768, WS_KV = 163840, WS_O = WS_KV + 16 * KVC,
              WS_XG = WS_O + 32 * 81920, WS_ST = WS_XG + 32 * 81920, WS_H = WS_ST + 32 * 8 * 64 * 64, WS_W1F = WS_H + 32 * 221184,
              WS_W2F = WS_W1F + 2160 * 1024, WS_END = WS_W2F + 2160 * 1024, WS_DBG = 20u << 20;
constexpr u32 L_H = 0, L_WQ = 82944, L_WKV = 0, L_XK = 25600, L_PP = 139264, L_WO = 0, L_RING = 102400, L_WRED = 102400, L_X = 0, L_XE = 81920, L_STP = 126976, L_PART = 0,
              L_SA = 152064, L_G1 = 154624, L_AUX = 157184, L_MRED = 161024, L_A = 161536, L_OWN = 161792, L_DUMMY = 163328, L_TOTAL = 163840;
constexpr int HS = 1296;
constexpr int NPRE = 13;
#define QD 6
#define WD 6
#define KVF_I 6
#define POLLW 3
#define PSLEEP 4
#define TOUCH_OWN 1
#define UA1 24
#define WOD 8
constexpr int WPC = 7;
constexpr u32 SPIN_CAP = 400000, ONE2 = 0x3F803F80u;
template <int N> struct IC { static constexpr int value = N; };

DI float bflo(u32 v) { return __uint_as_float(v << 16); }
DI float bfhi(u32 v) { return __uint_as_float(v & 0xffff0000u); }
DI u32 pk2(float a, float b) { f32x2 v = {a, b}; return __builtin_bit_cast(u32, __builtin_convertvector(v, bf16x2)); }
DI float bfr(float a) { return bflo(pk2(a, a)); }
DI f32x16 mfma32(v4u a, v4u b, f32x16 c) { return __builtin_amdgcn_mfma_f32_32x32x16_bf16(__builtin_bit_cast(bf16x8, a), __builtin_bit_cast(bf16x8, b), c, 0, 0, 0); }
DI f32x4 mfma16(v4u a, v4u b, f32x4 c) { return __builtin_amdgcn_mfma_f32_16x16x32_bf16(__builtin_bit_cast(bf16x8, a), __builtin_bit_cast(bf16x8, b), c, 0, 0, 0); }
DI void bar() { asm volatile("s_waitcnt lgkmcnt(0)" ::: "memory"); __builtin_amdgcn_s_barrier(); asm volatile("" ::: "memory"); }
DI void vm0() { asm volatile("s_waitcnt vmcnt(0)" ::: "memory"); }
template <int CTRL> DI u32 dppu(u32 v) { return (u32)__builtin_amdgcn_update_dpp(0, (int)v, CTRL, 0xf, 0xf, true); }
template <int CTRL> DI float dpp(float v) { return __uint_as_float(dppu<CTRL>(__float_as_uint(v))); }
constexpr int X1 = 0xB1, X2 = 0x4E, HM = 0x141, R4 = 0x124, R8 = 0x128;
DI float t16(float a, float b) { auto r = __builtin_amdgcn_permlane16_swap(__float_as_uint(a), __float_as_uint(b), false, false); return __uint_as_float(r[0]) + __uint_as_float(r[1]); }
DI float t32(float a, float b) { auto r = __builtin_amdgcn_permlane32_swap(__float_as_uint(a), __float_as_uint(b), false, false); return __uint_as_float(r[0]) + __uint_as_float(r[1]); }
DI float hi32(float a) { auto r = __builtin_amdgcn_permlane32_swap(__float_as_uint(a), __float_as_uint(a), false, false); return __uint_as_float(r[1]); }
DI float sum16(float v) { v += dpp<X1>(v); v += dpp<X2>(v); v += dpp<R4>(v); v += dpp<R8>(v); return v; }
DI u32 umax16(u32 v) { v = max(v, dppu<X1>(v)); v = max(v, dppu<X2>(v)); v = max(v, dppu<R4>(v)); v = max(v, dppu<R8>(v)); return v; }
DI u32 umax64(u32 v) { v = umax16(v); auto r = __builtin_amdgcn_permlane16_swap(v, v, false, false); v = max(r[0], r[1]); auto q = __builtin_amdgcn_permlane32_swap(v, v, false, false); return max(q[0], q[1]); }

DI float dot2(u32 a, u32 b, float c) { return __builtin_amdgcn_fdot2_f32_bf16(__builtin_bit_cast(bf16x2, a), __builtin_bit_cast(bf16x2, b), c, false); }
DI u32 rfl(u32 v) { return (u32)__builtin_amdgcn_readfirstlane((int)v); }
DI float rdl(float v, int l) { return __uint_as_float((u32)__builtin_amdgcn_readlane((int)__float_as_uint(v), l)); }
DI float ln_s(float var) { return bfr(sqrtf(bfr(bfr(var) + 1e-5f))); }

struct Args { const char *x, *wq, *wk, *wv, *wo, *w1, *b1, *w2, *b2, *c, *wmod; char* out; char* ws; };

__global__ __launch_bounds__(512, 2) void fused_block(Args a) {
    extern __shared__ __attribute__((aligned(16))) char lds_[];
    LAS char* lds = (LAS char*)lds_;
    const u32 ldsb = (u32)(size_t)lds;
    const int t = threadIdx.x, lane = t & 63, w = __builtin_amdgcn_readfirstlane(t >> 6);
    const int b = blockIdx.x, xcd = b & 7, j = (b >> 3) & 7, grp = xcd * 4 + (b >> 6), row0 = grp * 64, mi = b >> 3;
    const int hi = lane >> 5, g = lane >> 4, l15 = lane & 15, l31 = lane & 31, q4 = (lane & 15) >> 2, p4 = lane & 3;
    GAS char* ws = (GAS char*)a.ws;
    const __amdgpu_buffer_rsrc_t rs = __builtin_amdgcn_make_buffer_rsrc(a.ws, 0, WS_END, 0x00020000);
#define LD16(off) (*(const LAS v4u*)(lds + (off)))
#define LD8(off) (*(const LAS v2u*)(lds + (off)))
#define LDF4(off) (*(const LAS f32x4*)(lds + (off)))
#define ST16(off, v) (*(LAS v4u*)(lds + (off)) = (v))
#define ST8(off, v) (*(LAS v2u*)(lds + (off)) = (v))
#define STF4(off, v) (*(LAS f32x4*)(lds + (off)) = (v))
#define STF(off, v) (*(LAS float*)(lds + (off)) = (v))
#define LDF(off) (*(const LAS float*)(lds + (off)))
#define TR(off) __builtin_bit_cast(v2u, __builtin_amdgcn_ds_read_tr16_b64_v4i16((LAS s16x4*)(lds + (off))))
#define GL8(p) (*(const GAS v2u*)(p))
#define CLD16(off) __builtin_amdgcn_raw_buffer_load_b128(rs, (off), 0, 16)
#define CST16(off, v) __builtin_amdgcn_raw_buffer_store_b128((v), rs, (off), 0, 16)
#define CST8(off, v) __builtin_amdgcn_raw_buffer_store_b64((v), rs, (off), 0, 16)
#define AG16(d, vo, sb, imm) asm volatile("global_load_dwordx4 %0, %1, %2 offset:%3" : "=v"(d) : "v"(vo), "s"(sb), "n"(imm))
#define AG8(d, vo, sb, imm) asm volatile("global_load_dwordx2 %0, %1, %2 offset:%3" : "=v"(d) : "v"(vo), "s"(sb), "n"(imm))
#define AG2(d, vo, sb, imm) asm volatile("global_load_ushort %0, %1, %2 offset:%3" : "=v"(d) : "v"(vo), "s"(sb), "n"(imm))
#define AC16(d, vo, so, imm) asm volatile("buffer_load_dwordx4 %0, %1, %2, %3 offen offset:%4 sc1" : "=v"(d) : "v"(vo), "s"(rs), "s"(so), "n"(imm))
#define AC16R(d, vo, so, imm) asm volatile("buffer_load_dwordx4 %0, %1, %2, %3 offen offset:%4 sc1" : "+v"(d) : "v"(vo), "s"(rs), "s"(so), "n"(imm))
#define AC8(d, vo, so, imm) asm volatile("buffer_load_dwordx2 %0, %1, %2, %3 offen offset:%4 sc1" : "=v"(d) : "v"(vo), "s"(rs), "s"(so), "n"(imm))
#define DMA16(dst, vo, sb) asm volatile("s_mov_b32 m0, %0\n\ts_nop 0\n\tglobal_load_lds_dwordx4 %1, %2" :: "s"(ldsb + (dst)), "v"(vo), "s"(sb) : "m0", "memory")
#define DMA4(dst, vo, sb) asm volatile("s_mov_b32 m0, %0\n\ts_nop 0\n\tglobal_load_lds_dword %1, %2" :: "s"(ldsb + (dst)), "v"(vo), "s"(sb) : "m0", "memory")
#define DMA16I(sl, il, vo, ig, sb) do { u32 tv_; asm volatile("s_add_u32 m0, %1, %2\n\tv_add_u32 %0, %4, %3\n\tglobal_load_lds_dwordx4 %0, %5" : "=&v"(tv_) : "s"(sl), "n"(il), "v"(vo), "n"(ig), "s"(sb) : "m0", "scc", "memory"); } while (0)
#define WAITV(n, ...) asm volatile("s_waitcnt vmcnt(%c[cnt])" : __VA_ARGS__ : [cnt] "n"(n) : "memory")
#define TIE(...) asm volatile("" : __VA_ARGS__)
#define TS(i)
#define TC(i)
    auto tr2 = [&](u32 o0, u32 o1) { v2u x0 = TR(o0), x1 = TR(o1); return (v4u){x0.x, x0.y, x1.x, x1.y}; };
    TS(0); TC(30);
    if (mi >= 28) {
        u64 pc; u32 len; asm volatile("s_getpc_b64 %0\n.Lfb_code_a:\n\ts_mov_b32 %1, .Lfb_code_b-.Lfb_code_a" : "=s"(pc), "=s"(len));
        const u32 vo = min((u32)((mi - 28) * 512 + t) * 64u, len & ~3u);
        DMA4(L_DUMMY, vo, pc);
    }
    u32 Ev; { const u32 z = 0; asm volatile("global_load_dword %0, %1, %2 sc1" : "=v"(Ev) : "v"(z), "s"(a.ws)); }
    const int m3 = mi / 3, mr3 = mi - 3 * m3; const bool crit = mr3 == 0 && mi < 30, defer = mr3 != 0 && mi < 30;
    const int mcol = mr3 == 0 ? xcd * 160 + min(m3, 9) * 16 : 1280 + xcd * 320 + (min(m3, 9) * 2 + mr3 - 1) * 16;
    auto mod_load = [&](int col, v4u (&wm)[3], u32 (&cc)[3], int part) {
        const u32 mr = t >> 1, r2 = min(mr + 512u, 639u), co = col * 2 + (t & 1) * 16;
        if (part <= 0) { AG2(cc[0], mr * 2, a.c, 0); AG2(cc[1], mr * 2, a.c, 512); AG2(cc[2], r2 * 2, a.c, 0); AG16(wm[0], mr * 7680 + co, a.wmod, 0); }
        if (part < 0 || part == 1) AG16(wm[1], (mr + 256) * 7680 + co, a.wmod, 0);
        if (part < 0 || part == 2) AG16(wm[2], r2 * 7680 + co, a.wmod, 0);
    };
    auto mod_partial = [&](v4u (&wm)[3], u32 (&cc)[3]) {
        float acc[8] = {};
#pragma unroll
        for (int i = 0; i < 3; ++i) {
            const float cv = bflo(cc[i]); u32 sl = pk2(cv * __builtin_amdgcn_rcpf(1.f + __builtin_amdgcn_exp2f(cv * -1.4426950408889634f)), 0.f);
            if (i == 2 && t >= 256) sl = 0u;
            const u32 sh = sl << 16;
#pragma unroll
            for (int e = 0; e < 4; ++e) { acc[2 * e] = dot2(wm[i][e], sl, acc[2 * e]); acc[2 * e + 1] = dot2(wm[i][e], sh, acc[2 * e + 1]); }
        }
#pragma unroll
        for (int i = 0; i < 4; ++i) acc[i] = t32(acc[i], acc[i + 4]);
#pragma unroll
        for (int i = 0; i < 2; ++i) { acc[i] = t16(acc[i], acc[i + 2]); acc[i] += dpp<X2>(acc[i]); acc[i] += dpp<R4>(acc[i]); acc[i] += dpp<R8>(acc[i]); }
        if ((lane & 14) == 0) *(LAS f32x2*)(lds + L_MRED + (w * 16 + 8 * (lane & 1) + 4 * hi + 2 * (g & 1)) * 4) = (f32x2){acc[0], acc[1]};
    };
    v4u wm[3], xr[10]; u32 cc[3];
    const int tq = j >> 1, isV = j & 1; float kn2 = 0;
    const u32 l16 = lane * 16;
    if (crit) { mod_load(mcol, wm, cc, -1); __builtin_amdgcn_s_barrier(); }
    { const u32 vo = (row0 + 8 * w) * 1280 + l16, vt = (row0 + 8 * w + 2 * g) * 1280 + 1024 + l15 * 16;
#pragma unroll
      for (int i = 0; i < 8; ++i) AG16(xr[i], vo + (i / 3) * 3840, a.x, (i % 3) * 1280);
      AG16(xr[8], vt, a.x, 0); AG16(xr[9], vt, a.x, 1280); }
    v4u wm2[3]; u32 cc2[3];
    if (defer) mod_load(mcol, wm2, cc2, -1);
    const u32 slice_vo = ((u32)lane / 10u) * 1120 + l16 + j * 160 + w * 7680, slice_sl = ldsb + w * 960;
#define SLICE(W, dst, k) do { SB(); if ((k) < 13) DMA16I(slice_sl, (dst) + (k) * 7680, slice_vo, (k) * 61440, W); \
                              else if (w < 2 || (w == 2 && lane < 40)) DMA16I(slice_sl, (dst) + (k) * 7680, slice_vo, (k) * 61440, W); SB(); } while (0)
    auto mod_publish = [&](int col, u32 tg) {
        if (lane < 8) { float s0 = 0, s1 = 0;
#pragma unroll
            for (int i = 0; i < 8; ++i) { const f32x2 v = *(const LAS f32x2*)(lds + L_MRED + (i * 16 + 2 * lane) * 4); s0 += v.x; s1 += v.y; }
            CST8(WS_MODG + (col / 2 + lane) * 8, ((v2u){pk2(s0, s1), tg})); } };
    TS(23);
    if (crit) { WAITV(10, "+v"(Ev), "+v"(wm[0]), "+v"(wm[1]), "+v"(wm[2]), "+v"(cc[0]), "+v"(cc[1]), "+v"(cc[2])); mod_partial(wm, cc); bar(); if (w == 0) mod_publish(mcol, rfl(Ev) * 1664525u + 1013904223u); }
    TS(1);
    SLICE(a.wq, L_WQ, 0); SLICE(a.wq, L_WQ, 1);
    if (defer) WAITV(8); else WAITV(2);
    TIE("+v"(Ev), "+v"(xr[0]), "+v"(xr[1]), "+v"(xr[2]), "+v"(xr[3]), "+v"(xr[4]), "+v"(xr[5]), "+v"(xr[6]), "+v"(xr[7]), "+v"(xr[8]), "+v"(xr[9]));
    const u32 tag = rfl(Ev) * 1664525u + 1013904223u;
    v4u mg; const u32 mgo = min(t, 319) * 16;
    f32x2 xn[40];
    {
        float s[10], q[10];
        auto row = [&](int i) { s[i] = 0; q[i] = 0;
#pragma unroll
            for (int e = 0; e < 4; ++e) { s[i] = dot2(xr[i][e], ONE2, s[i]); q[i] = dot2(xr[i][e], xr[i][e], q[i]); } };
        row(0); row(1); SLICE(a.wq, L_WQ, 2); row(2); row(3); row(4); SLICE(a.wq, L_WQ, 3); row(5); row(6); row(7); SLICE(a.wq, L_WQ, 4); row(8); row(9);
#pragma unroll
        for (int i = 0; i < 4; ++i) { s[i] = t32(s[i], s[i + 4]); q[i] = t32(q[i], q[i + 4]); }
        SLICE(a.wq, L_WQ, 5);
        float rsd[2], nb[2];
#pragma unroll
        for (int i = 0; i < 2; ++i) { const float ss = sum16(t16(s[i], s[i + 2]) + s[8 + i]), qq = sum16(t16(q[i], q[i + 2]) + q[8 + i]);
            const float m = ss * (1.f / 640.f); rsd[i] = __builtin_amdgcn_rcpf(ln_s(fmaxf(qq * (1.f / 640.f) - m * m, 0.f))); nb[i] = -bfr(m) * rsd[i]; }
        auto nrm = [&](int i) { const float r_ = i < 8 ? rdl(rsd[i & 1], 16 * (i >> 1)) : rsd[i & 1], n_ = i < 8 ? rdl(nb[i & 1], 16 * (i >> 1)) : nb[i & 1];
#pragma unroll
            for (int e = 0; e < 4; ++e) xn[4 * i + e] = (f32x2){bflo(xr[i][e]), bfhi(xr[i][e])} * r_ + n_;
            TIE("+v"(xn[4 * i]), "+v"(xn[4 * i + 1]), "+v"(xn[4 * i + 2]), "+v"(xn[4 * i + 3])); };
        { const char* kp = isV ? a.wv : a.wk; const u32 sl = ldsb + w * 1024, vo = t * 16;
          DMA16I(sl, L_WKV, vo, 0, kp); nrm(0); DMA16I(sl, L_WKV + 8192, vo, 8192, kp); nrm(1); DMA16I(sl, L_WKV + 16384, vo, 16384, kp); if (w == 0) DMA16I(sl, L_WKV + 24576, vo, 24576, kp); }
        nrm(2); nrm(3); SLICE(a.wq, L_WQ, 6); nrm(4); nrm(5); nrm(6); SLICE(a.wq, L_WQ, 7); nrm(7); nrm(8); nrm(9); if (lane < 60) SLICE(a.wq, L_WQ, 8);
    }
    if ((w >> 1) == tq) {
#pragma unroll
        for (int i = 0; i < 10; ++i) { v4u o;
#pragma unroll
            for (int e = 0; e < 4; ++e) o[e] = pk2(xn[4 * i + e].x, xn[4 * i + e].y);
            ST16(i < 8 ? L_XK + (8 * (w & 1) + i) * HS + l16 : L_XK + (8 * (w & 1) + 2 * g + i - 8) * HS + 1024 + l15 * 16, o); } }
    if (defer) { WAITV(12, "+v"(wm2[0]), "+v"(wm2[1]), "+v"(wm2[2]), "+v"(cc2[0]), "+v"(cc2[1]), "+v"(cc2[2])); mod_partial(wm2, cc2); }
    TS(2);
    WAITV(3); bar();
    AC16(mg, mgo, WS_MODG, 0);
    TS(3);
    const int hh = w & 3, th = w >> 2, ks2 = min(w + 16, 19);
    v4u wf[40], kf[3][2]; f32x2 xk[12];
    { const u32 wb = L_WKV + (8 * g + q4) * 40 + p4 * 8;
#pragma unroll
      for (int i = 0; i < 3; ++i) { const u32 o = wb + (i < 2 ? w + 8 * i : ks2) * 1280;
          const v4u xv = LD16(L_XK + l15 * HS + g * 16 + (i < 2 ? w + 8 * i : ks2) * 64);
          kf[i][0] = tr2(o, o + 160); kf[i][1] = tr2(o + 32, o + 192);
#pragma unroll
          for (int e = 0; e < 4; ++e) xk[4 * i + e] = (f32x2){bflo(xv[e]), bfhi(xv[e])};
          TIE("+v"(xk[4 * i]), "+v"(xk[4 * i + 1]), "+v"(xk[4 * i + 2]), "+v"(xk[4 * i + 3])); } }
    const u32 wqb = L_WQ + (8 * hi + q4) * 160 + (20 * hh + 16 * (g & 1) + 4 * p4) * 2; u32 wq2 = wqb + 20 * 2560; TIE("+v"(wq2));
    auto wq_frag = [&](int ks) { const int k = ks < 27 ? ks : ks - 27; const u32 o = k < 20 ? wqb + k * 2560 : wq2 + (k - 20) * 2560; wf[ks] = tr2(o, o + 640); };
#pragma unroll
    for (int ks = 0; ks < NPRE; ++ks) wq_frag(ks);
    WAITV(0, "+v"(mg));
    if (defer && w == 7) mod_publish(mcol, tag);
    TS(26);
    if (w < 5) {
        for (u32 sp = 0; sp < SPIN_CAP; ++sp) { if (__all(mg.y == tag && mg.w == tag)) break; AC16(mg, mgo, WS_MODG, 0); WAITV(0, "+v"(mg)); }
        f32x4 f = {bflo(mg.x), bfhi(mg.x), bflo(mg.z), bfhi(mg.z)};
        if (t >= 160) { const u32 p0 = pk2(1.f + f.x, 1.f + f.y), p1 = pk2(1.f + f.z, 1.f + f.w); f = (f32x4){bflo(p0), bfhi(p0), bflo(p1), bfhi(p1)}; }
        STF4(L_SA + t * 16, f);
    }
    bar();
    TS(4);
    {
        f32x4 acc0 = {}, acc1 = {}, gv[6], sv[6];
#pragma unroll
        for (int i = 0; i < 6; ++i) { const u32 mo = (i < 4 ? w + 8 * (i >> 1) : ks2) * 128 + g * 32 + (i & 1) * 16; gv[i] = LDF4(L_G1 + mo); sv[i] = LDF4(L_SA + mo); }
        SB();
#pragma unroll
        for (int i = 0; i < 3; ++i) { v4u hf;
#pragma unroll
            for (int e = 0; e < 4; ++e) { const f32x4 gg = gv[2 * i + (e >> 1)], ss = sv[2 * i + (e >> 1)]; const f32x2 v = xk[4 * i + e] * (e & 1 ? gg.zw : gg.xy) + (e & 1 ? ss.zw : ss.xy); hf[e] = pk2(v.x, v.y); }
            if (i == 2 && w >= 4) hf = (v4u){};
            if (isV) { acc0 = mfma16(hf, kf[i][0], acc0); acc1 = mfma16(hf, kf[i][1], acc1); } else { acc0 = mfma16(kf[i][0], hf, acc0); acc1 = mfma16(kf[i][1], hf, acc1); } }
        STF4(L_WKV + w * 1280 + l16, acc0);
        if (isV ? l15 < 4 : g == 0) STF4(L_WKV + w * 1280 + 1024 + (isV ? 4 * g + l15 : l15) * 16, acc1);
    }
    const int key0 = row0 + 16 * tq; const u32 cb = WS_KV + (key0 >> 7) * KVC, tile = (key0 >> 5) & 3, kk = (key0 & 31) + l15, vb = cb + 4096 + tile * 1536 + (tq & 1) * 768;
    u32 kvs0 = isV ? vb + (g & 1) * 384 + l15 * 16 + 8 * (g >> 1) : cb + tile * 1024 + (kk + 32 * (g & 1)) * 16 + 8 * (g >> 1);
    u32 kvs1 = isV ? vb + (q4 & 1) * 384 + (16 + 4 * g + p4) * 16 + 8 * (q4 >> 1) : cb + 10240 + tile * 512 + kk * 16, kvr = L_WKV + g * 1280 + 1024 + l15 * 16;
    TIE("+v"(kvs0), "+v"(kvs1), "+v"(kvr));
    bar();
    TS(5);
    if (w == 0) {
        __builtin_amdgcn_s_setprio(3);
        f32x4 r0[6];
#pragma unroll
        for (int k = 0; k < 6; ++k) r0[k] = LDF4(L_WKV + k * 1280 + l16);
        SB();
        r0[0] += r0[1]; r0[2] += r0[3]; r0[1] = LDF4(L_WKV + 6 * 1280 + l16); r0[3] = LDF4(L_WKV + 7 * 1280 + l16); SB();
        r0[0] += r0[4]; r0[2] += r0[5];
        f32x4 s1 = LDF4(kvr), s2 = LDF4(kvr + 5120); SB();
        const f32x4 s0 = (r0[0] + r0[1]) + (r0[2] + r0[3]);
        const v2u p0 = {pk2(s0.x, s0.y), pk2(s0.z, s0.w)};
        CST8(kvs0, p0); SB();
        s1 += s2;
        const float u0 = t32(s1.x, s1.y), u1 = t32(s1.z, s1.w), u2 = t16(u0, u1);
        const auto uy = __builtin_amdgcn_permlane32_swap(__float_as_uint(u2), __float_as_uint(u2), false, false);
        const u32 pa = pk2(__uint_as_float(uy[0]), __uint_as_float(uy[1]));
        const auto pz = __builtin_amdgcn_permlane16_swap(pa, pa, false, false);
        v2u p1 = {pz[0], pz[1]};
        if (isV) {
            if (g) { const u32 c = p4 == 0 ? ONE2 : 0u; p1 = (v2u){c, c}; }
            if (g < 2) CST8(kvs1, p1);
        } else {
            if (g == 0) CST16(kvs1, ((v4u){p1.x, p1.y, ONE2, ONE2}));
            kn2 = dot2(p0.x, p0.x, dot2(p0.y, p0.y, 0.f));
            kn2 = t16(kn2, kn2); kn2 = t32(kn2, kn2);
            kn2 = dot2(p1.x, p1.x, dot2(p1.y, p1.y, kn2));
            kn2 = __uint_as_float(umax16(__float_as_uint(kn2)));
        }
    }
    TS(6);
    {
        const u32 hb0 = L_H + 8 * w * HS + l16, ht0 = L_H + (8 * w + 2 * g) * HS + 1024 + l15 * 16;
        f32x4 gm[2], sm[2], gt[2], st[2];
#pragma unroll
        for (int k = 0; k < 2; ++k) { gm[k] = LDF4(L_G1 + lane * 32 + k * 16); sm[k] = LDF4(L_SA + lane * 32 + k * 16); }
        SLICE(a.wq, L_WQ - 69120, 9); SLICE(a.wq, L_WQ - 69120, 10);
#pragma unroll
        for (int i = 0; i < 10; ++i) { v4u o;
            if (i == 4) {
#pragma unroll
                for (int k = 0; k < 2; ++k) { gt[k] = LDF4(L_G1 + 2048 + l15 * 32 + k * 16); st[k] = LDF4(L_SA + 2048 + l15 * 32 + k * 16); }
                SLICE(a.wq, L_WQ - 69120, 12); SLICE(a.wq, L_WQ - 69120, 13); }
            if (i == 2) SLICE(a.wq, L_WQ - 69120, 11);
#pragma unroll
            for (int e = 0; e < 4; ++e) { const f32x4 gg = i < 8 ? gm[e >> 1] : gt[e >> 1], ss = i < 8 ? sm[e >> 1] : st[e >> 1];
                const f32x2 v = xn[4 * i + e] * (e & 1 ? gg.zw : gg.xy) + (e & 1 ? ss.zw : ss.xy); o[e] = pk2(v.x, v.y); }
            if (i == KVF_I && w == 0) { WAITV(5); if (lane == 0) CST8(WS_KVF + b * 8, ((v2u){__float_as_uint(kn2), tag})); __builtin_amdgcn_s_setprio(0); TS(7); }
            ST16(i >= 8 ? ht0 + (i - 8) * HS : hb0 + i * HS, o); SB(); }
    }
    if (w == 0) WAITV(1); else WAITV(0);
    bar();
    TS(24);
    u32 ppd[3], ppv[3]; v4u ppf[3];
    const bool pp3 = w == 1 && b < 224, pp3w2 = b >= 112;
    auto pp_addr = [&](int r) { const int i = r < 2 ? b * 8 + w : 2048 + min(pp3w2 ? b - 112 : b, 111); const bool isw2 = r < 2 ? r == 1 : pp3w2;
        if (isw2) { const int ks = i / 40, nt = i - ks * 40; ppv[r] = ks * 40960 + nt * 32 + (lane >> 1) * 1280 + (lane & 1) * 16; ppd[r] = WS_W2F + (nt * 54 + ks) * 1024; }
        else { const int ks = i / 54, nt = i - ks * 54, jj = ks / 5, ii = ks - jj * 5, lr = lane >> 2;
            ppv[r] = (80 * jj + (ii < 4 ? 32 * (ii >> 1) + 8 * (ii & 1) : 64)) * 3456 + nt * 64 + (lr + (ii < 4 ? (lr & 8) : 0)) * 3456 + p4 * 16; ppd[r] = WS_W1F + (nt * 40 + ks) * 1024; }
        ppd[r] = rfl(ppd[r]); TIE("+v"(ppv[r]), "+s"(ppd[r])); };
    auto pp_load = [&](int r) { DMA16(L_PP + (r < 2 ? w + 8 * r : 16) * 1024, ppv[r], (r < 2 ? r == 1 : pp3w2) ? a.w2 : a.w1); };
    auto pp_read = [&](int r) { const bool isw2 = r < 2 ? r == 1 : pp3w2;
        const u32 o = L_PP + (r < 2 ? w + 8 * r : 16) * 1024 + (isw2 ? (16 * (g >> 1) + 4 * (g & 1) + q4) * 32 + p4 * 8 : (4 * hi + q4) * 64 + (g & 1) * 32 + p4 * 8);
        ppf[r] = tr2(o, o + (isw2 ? 256 : 512)); };
    auto pp_store = [&](int r) { __builtin_amdgcn_raw_buffer_store_b128(ppf[r], rs, l16, ppd[r], 16); };
    v4u qf0, qf1;
#define TOUCH(vo, so) asm volatile("s_mov_b32 m0, %0\n\ts_nop 0\n\tbuffer_load_dword %1, %2, %3 offen lds" :: "s"(ldsb + L_DUMMY), "v"((u32)(vo)), "s"(rs), "s"(so) : "m0", "memory")
    const u32 kvo0 = t * 16;
#define KVDMA(sl, il, sg, ig) do { u32 ts_; asm volatile("s_add_u32 m0, %1, %2\n\ts_add_u32 %0, %3, %4\n\tbuffer_load_dwordx4 %5, %6, %0 offen sc1 lds" : "=&s"(ts_) : "s"(sl), "n"(il), "s"(sg), "n"(ig), "v"(l16), "s"(rs) : "m0", "scc", "memory"); } while (0)
    {
        f32x16 acc = {}; v4u hq[QD];
        const u32 hb = L_H + (32 * th + l31) * HS + hi * 16;
#pragma unroll
        for (int ks = 0; ks < QD; ++ks) hq[ks] = LD16(hb + ks * 32);
#pragma unroll
        for (int ks = 0; ks < 40; ++ks) {
            SB(); acc = mfma32(wf[ks], hq[ks % QD], acc); SB();
            if (ks + QD < 40) hq[ks % QD] = LD16(hb + (ks + QD) * 32);
            if (ks + WD >= NPRE && ks + WD < 40) wq_frag(ks + WD);
            if (ks == 1 || ks == 3 || ks == 5) pp_addr(ks >> 1);
        }
        SB();
        const float sc = 0.22360679774997896f * 1.4426950408889634f;
        qf0 = (v4u){pk2(acc[0] * sc, acc[1] * sc), pk2(acc[2] * sc, acc[3] * sc), pk2(acc[4] * sc, acc[5] * sc), pk2(acc[6] * sc, acc[7] * sc)};
        qf1 = (v4u){hi ? 0u : pk2(acc[8] * sc, acc[9] * sc), hi ? 0u : pk2(acc[10] * sc, acc[11] * sc), 0u, 0u};
        float qn2 = dot2(qf0.x, qf0.x, dot2(qf0.y, qf0.y, dot2(qf0.z, qf0.z, dot2(qf0.w, qf0.w, dot2(qf1.x, qf1.x, dot2(qf1.y, qf1.y, 0.f))))));
        qn2 = t32(qn2, qn2);
        TS(8);
        v4u kv0[6];
        if (w < 4) {
            v4u f0, f1;
            for (u32 sp = 0; sp < SPIN_CAP; ++sp) { f0 = CLD16(WS_KVF + l16); f1 = CLD16(WS_KVF + 1024 + l16);
                if (__all(f0.y == tag && f0.w == tag && f1.y == tag && f1.w == tag)) break;
                __builtin_amdgcn_s_sleep(PSLEEP); }
#pragma unroll
            for (int i = 0; i < 6; ++i) AC16(kv0[i], kvo0, WS_KV + i * 4096, 0);
            const u32 km = umax64(max(max(f0.x, f0.z), max(f1.x, f1.z)));
            if (t == 0) STF(L_A, __uint_as_float(km));
        }
        TS(9);
        bar();
        if (w < 4) {
            WAITV(3, "+v"(kv0[0]), "+v"(kv0[1]), "+v"(kv0[2]));
#pragma unroll
            for (int i = 0; i < 3; ++i) ST16(L_RING + i * 4096 + kvo0, kv0[i]);
            WAITV(0, "+v"(kv0[3]), "+v"(kv0[4]), "+v"(kv0[5]));
#pragma unroll
            for (int i = 3; i < 6; ++i) ST16(L_RING + i * 4096 + kvo0, kv0[i]);
        }
        const float kmax2 = LDF(L_A);
        const float Msh = bfr(__builtin_amdgcn_sqrtf(qn2 * kmax2) * 1.01f + 1e-30f);
        qf1.z = hi ? 0u : (pk2(-Msh, 0.f) & 0xffffu);
        TS(25);
    }
    bar();
    if (b == 0 && t == 128) __hip_atomic_store((GAS u32*)(ws + WS_E), tag, RLX_AGENT);
    TS(10); TC(28);
    const int ntile = j < 6 ? 7 : 6, nt0 = j < 6 ? 7 * j : 42 + 6 * (j - 6); const bool has_tile = w < ntile;
    const u32 w1o = WS_W1F + (nt0 + min(w, ntile - 1)) * 40960;
    v4u w1f[40];
    f32x16 oacc = {};
    {
        const u32 ak0 = L_RING + lane * 16, ak1 = L_RING + 10240 + l31 * 16, av = L_RING + 4096 + (hi * 24 + (l31 < 24 ? l31 : (l31 < 28 ? l31 - 4 : l31 - 12))) * 16;
        v4u k0 = LD16(ak0), k1 = LD16(ak1);
        f32x16 sc = mfma32(k0, qf0, (f32x16){}); sc = mfma32(k1, qf1, sc);
        k0 = LD16(ak0 + 1024); k1 = LD16(ak1 + 512);
        v4u p1 = {}, v1 = {}, pf0, pf1;
        const u32 kvl = ldsb + L_RING + w * 1024, kvg = WS_KV + w * 1024, tw2 = WS_W2F + mi * 71680 + w * 1280;
#define EX(i) __builtin_amdgcn_exp2f(sc[i])
        const int mu = t - 320; const u32 mgo2 = (t < 320 ? 1920 + 4 * t : (mu < 20 ? 1280 + 80 * j + 4 * mu : 3200 + 80 * j + 4 * (min(mu, 39) - 20))) * 4;
        auto chunk = [&](auto CC) {
            constexpr int c = decltype(CC)::value; constexpr u32 cur = (c % 3) * KVC, nxt = ((c + 1) % 3) * KVC, fre = ((c + 2) % 3) * KVC;
            constexpr int NY = c == 2 ? 4 : (c < 7 ? 2 : (c == 7 ? 1 : 5));
#pragma unroll
            for (int i = 0; i < 4; ++i) {
                v4u p0; f32x16 sn; float e[16];
                const u32 kb = i < 2 ? cur + (i + 2) * 1024 : nxt + (i - 2) * 1024, kb1 = i < 2 ? cur + (i + 2) * 512 : nxt + (i - 2) * 512;
                sn = mfma32(k0, qf0, (f32x16){}); SB();
                const v4u v0 = LD16(cur + av + i * 1536); k0 = LD16(kb + ak0); SB();
                e[0] = EX(0); e[1] = EX(1); e[2] = EX(2); e[3] = EX(3); SB();
                oacc = mfma32(v1, p1, oacc); SB();
                v1 = LD16(cur + av + i * 1536 + 768); SB();
                e[4] = EX(4); e[5] = EX(5); SB(); p0.x = pk2(e[0], e[1]); p0.y = pk2(e[2], e[3]); SB(); e[6] = EX(6); SB();
                sn = mfma32(k1, qf1, sn); SB();
                k1 = LD16(kb1 + ak1); SB();
                e[7] = EX(7); e[8] = EX(8); SB(); p0.z = pk2(e[4], e[5]); SB(); e[9] = EX(9); SB(); p0.w = pk2(e[6], e[7]); SB(); e[10] = EX(10); e[11] = EX(11); SB();
                oacc = mfma32(v0, p0, oacc); SB();
                e[12] = EX(12); e[13] = EX(13); SB(); p1.x = pk2(e[8], e[9]); SB(); e[14] = EX(14); e[15] = EX(15); SB(); p1.y = pk2(e[10], e[11]); p1.z = pk2(e[12], e[13]); p1.w = pk2(e[14], e[15]);
                TIE("+v"(p1));
                if (i == 0) {
                    if constexpr (c == 4) { if (t == 64) CST8(WS_WF + b * 8, ((v2u){0u, tag})); }
                    if constexpr (c == WPC) { if (w == 7) { AC16(pf0, l16, WS_WF, 0); AC16(pf1, l16, WS_WF, 1024); } }
                    if constexpr (c < 14) { if (w < 4) KVDMA(kvl, fre + 8192, kvg, (c + 2) * KVC + 8192); KVDMA(kvl, fre, kvg, (c + 2) * KVC); }
                    if constexpr (c == 2) { pp_read(0); pp_read(1); pp_read(2); }
                }
                if (i == 1) {
                    if constexpr (c == 2) { pp_store(0); pp_store(1); if (pp3) pp_store(2); }
                    if constexpr (c == 14) AC16(mg, mgo2, WS_MODG, 0);
                }
                if (i == 3) { if constexpr (c == WPC) { if (w == 7) {
                    WAITV(2, "+v"(pf0), "+v"(pf1));
                    for (u32 sp = 0; sp < SPIN_CAP; ++sp) { if (__all(pf0.y == tag && pf0.w == tag && pf1.y == tag && pf1.w == tag)) break;
                        __builtin_amdgcn_s_sleep(4); AC16(pf0, l16, WS_WF, 0); AC16(pf1, l16, WS_WF, 1024); WAITV(0, "+v"(pf0), "+v"(pf1)); } } } }
                if (i >= 2) {
                    if constexpr (c == 0) { pp_load(i - 2); if (i == 3 && pp3) pp_load(2); }
                    if constexpr (c >= 1 && c < 8) { if (i == 2) SLICE(a.wo, L_WO, 2 * c - 2); else SLICE(a.wo, L_WO, 2 * c - 1); }
                    if constexpr (c > WPC) { const int k = 4 * (c - WPC - 1) + 2 * (i - 2); AC16(w1f[k], l16, w1o + (k & ~3) * 1024, (k & 3) * 1024); AC16(w1f[k + 1], l16, w1o + (k & ~3) * 1024, ((k + 1) & 3) * 1024); }
                    if constexpr (c > WPC && c < WPC + 8) { if (i == 3 && lane < 10) TOUCH(l16 * 8, tw2 + (c - WPC - 1) * 10240); }
                }
                SB();
                sc = sn;
            }
            if constexpr (c < 14) WAITV(NY);
            bar();
        };
        chunk(IC<0>{}); chunk(IC<1>{}); chunk(IC<2>{}); chunk(IC<3>{}); chunk(IC<4>{}); chunk(IC<5>{}); chunk(IC<6>{}); chunk(IC<7>{});
        chunk(IC<8>{}); chunk(IC<9>{}); chunk(IC<10>{}); chunk(IC<11>{}); chunk(IC<12>{}); chunk(IC<13>{}); chunk(IC<14>{}); chunk(IC<15>{});
        oacc = mfma32(v1, p1, oacc);
    }
    TS(11); TC(29);
    {
    int t2 = threadIdx.x; TIE("+v"(t2));
    const int t = t2, lane = t & 63, hi = lane >> 5, g = lane >> 4, l15 = lane & 15, l31 = lane & 31, q4 = (lane & 15) >> 2, p4 = lane & 3; const u32 l16 = lane * 16;
    float lsum = hi32(oacc[8]);
    if (__any(!(lsum >= 1e-12f && lsum < 3e38f))) {
        qf1.z = 0u;
        const u32 ak0 = WS_KV + lane * 16, ak1 = WS_KV + 10240 + l31 * 16, av = WS_KV + 4096 + (hi * 24 + (l31 < 24 ? l31 : 20)) * 16;
        float mx = -__builtin_inff();
        for (int tI = 0; tI < 64; ++tI) { const u32 cb = (tI >> 2) * KVC, i = tI & 3;
            const v4u k0 = CLD16(cb + ak0 + i * 1024), k1 = CLD16(cb + ak1 + i * 512);
            f32x16 s = mfma32(k0, qf0, (f32x16){}); s = mfma32(k1, qf1, s);
#pragma unroll
            for (int r = 0; r < 16; ++r) mx = fmaxf(mx, s[r]); }
        mx = fmaxf(mx, __shfl_xor(mx, 32));
        oacc = (f32x16){};
        for (int tI = 0; tI < 64; ++tI) { const u32 cb = (tI >> 2) * KVC, i = tI & 3;
            const v4u k0 = CLD16(cb + ak0 + i * 1024), k1 = CLD16(cb + ak1 + i * 512);
            const v4u v0 = CLD16(cb + av + i * 1536), v1 = CLD16(cb + av + i * 1536 + 768);
            f32x16 s = mfma32(k0, qf0, (f32x16){}); s = mfma32(k1, qf1, s);
            v4u p0, p1;
#pragma unroll
            for (int e = 0; e < 4; ++e) { p0[e] = pk2(__builtin_amdgcn_exp2f(s[2 * e] - mx), __builtin_amdgcn_exp2f(s[2 * e + 1] - mx));
                                          p1[e] = pk2(__builtin_amdgcn_exp2f(s[8 + 2 * e] - mx), __builtin_amdgcn_exp2f(s[9 + 2 * e] - mx)); }
            oacc = mfma32(v0, p0, oacc); oacc = mfma32(v1, p1, oacc); }
        lsum = hi32(oacc[8]);
    }
    const u32 gfl = WS_GF + grp * 1024;
    const int mp = w >> 1, par = w & 1;
    v2u xres[3]; u32 b1v;
    {
        const float il = __builtin_amdgcn_rcpf(lsum); const int mt = 2 * th + (l31 >> 4);
        const v4u o0 = {pk2(oacc[0] * il, oacc[1] * il), pk2(oacc[2] * il, oacc[3] * il), pk2(oacc[4] * il, oacc[5] * il), pk2(oacc[6] * il, oacc[7] * il)};
        const u32 ob = WS_O + grp * 81920 + mt * 20480 + l15 * 16;
        CST16(ob + (2 * j + (hh >> 1)) * 1024 + (2 * (hh & 1) + hi) * 256, o0);
        const int ci = 64 + 2 * j + (hh >> 1);
        if (!hi) CST8(ob + ci * 256 + 8 * (hh & 1), ((v2u){pk2(oacc[8] * il, oacc[9] * il), pk2(oacc[10] * il, oacc[11] * il)}));
        SB();
        const u32 xo = (row0 + 16 * mp + l15) * 1280 + j * 160 + g * 8;
        AG8(xres[0], xo + par * 64, a.x, 0); AG8(xres[1], xo + par * 64, a.x, 32); AG8(xres[2], xo, a.x, 128);
        AG2(b1v, ((nt0 + min(w, ntile - 1)) * 32 + l31) * 2, a.b1, 0);
    }
    {
        WAITV(6, "+v"(mg));
        if (t == 511) STF(L_MRED, 0.f);
        const int mu = t - 320; const u32 mgo2 = (t < 320 ? 1920 + 4 * t : (mu < 20 ? 1280 + 80 * j + 4 * mu : 3200 + 80 * j + 4 * (min(mu, 39) - 20))) * 4;
                        if (w < 6) {
                            for (u32 sp = 0; sp < SPIN_CAP; ++sp) { if (__all(mg.y == tag && mg.w == tag)) break; AC16(mg, mgo2, WS_MODG, 0); WAITV(0, "+v"(mg)); }
                            v2u v = {mg.x, mg.z}; f32x4 f = {bflo(v.x), bfhi(v.x), bflo(v.y), bfhi(v.y)};
                            if (w < 5) {
                                const int isg = t >= 160, tp = t - 160 * isg, jj = (tp * 205) >> 12, r4 = tp - jj * 20;
                                if (isg) { v = (v2u){pk2(1.f + f.x, 1.f + f.y), pk2(1.f + f.z, 1.f + f.w)}; f = (f32x4){bflo(v.x), bfhi(v.x), bflo(v.y), bfhi(v.y)}; }
                                const int o = r4 < 16 ? (r4 >> 3) * 64 + (r4 & 3) * 16 + (r4 & 4) * 2 : 128 + (r4 & 1) * 16 + (r4 & 2) * 4;
                                ST8(L_AUX + (isg ? 0 : 1280) + jj * 160 + o, v);
                                if (jj == j) { if (isg) STF4(L_OWN + 320 + r4 * 16, f); ST8(L_OWN + (isg ? 960 : 1120) + r4 * 8, v); }
                            } else if (mu < 40) STF4(L_OWN + (mu < 20 ? mu * 16 : 640 + (mu - 20) * 16), f);
                        } else if (t < 464) ST16(L_AUX + 2560 + (t - 384) * 16, ((v4u){ONE2, ONE2, ONE2, ONE2}));
    }
#define W1T(k) "+v"(w1f[k]), "+v"(w1f[k + 1]), "+v"(w1f[k + 2]), "+v"(w1f[k + 3]), "+v"(w1f[k + 4]), "+v"(w1f[k + 5]), "+v"(w1f[k + 6]), "+v"(w1f[k + 7])
    WAITV(4, W1T(0), W1T(8)); TIE(W1T(16), W1T(24));
    if (lane < 12) DMA4(L_DUMMY, WS_O + grp * 81920 + (2 * th + (lane & 1)) * 20480 + (lane < 8 ? (2 * j + (hh >> 1)) * 1024 + (hh & 1) * 512 + (lane >> 1) * 128 : (64 + 2 * j + (hh >> 1)) * 256 + (lane & 2) * 64), a.ws);
    bar();
    if (t == 0) __hip_atomic_store((GAS u32*)(ws + gfl + j * 128), tag, RLX_AGENT);
    TS(12);
    auto wait_flags = [&](int phase, int first, int n) {
        for (u32 sp = 0; sp < SPIN_CAP; ++sp) { const u32 f = __builtin_amdgcn_raw_buffer_load_b32(rs, gfl + phase * 32768 + (first + (lane & (n - 1))) * 128, 0, 16);
            if (__all(f == tag)) break; __builtin_amdgcn_s_sleep(1); } };
    const u32 ab = L_AUX + min(l31, 2) * 1280 + hi * 16;
    float up[3];
    auto ua_part = [&](auto K0, auto K1) { constexpr int k0 = decltype(K0)::value, k1 = decltype(K1)::value; f32x16 ua = {}; v4u af[4];
#pragma unroll
        for (int ks = k0; ks < k0 + 4; ++ks) af[ks & 3] = LD16(ab + ks * 32);
#pragma unroll
        for (int ks = k0; ks < k1; ++ks) { SB(); ua = mfma32(af[ks & 3], w1f[ks], ua); SB(); if (ks + 4 < k1) af[ks & 3] = LD16(ab + (ks + 4) * 32); }
#pragma unroll
        for (int i = 0; i < 3; ++i) up[i] = k0 ? up[i] + ua[i] : ua[i];
        TIE("+v"(up[0]), "+v"(up[1]), "+v"(up[2])); };
    ua_part(IC<0>{}, IC<UA1>{});
    u32 x1p[6];
    {
        v4u xf[10], wr[WOD]; f32x4 acc[5] = {};
        const u32 wm_ = L_WO + (320 * par + 20 * (g >> 1) + 4 * (g & 1) + q4) * 160 + p4 * 8, wl_ = L_WO + (320 * par + 40 * g + 16 + q4) * 160 + p4 * 8;
        auto wo_frag = [&](int s_) { const int i = s_ / 5, nt = s_ % 5; const u32 o = i < 8 ? wm_ + i * 6400 + nt * 32 : wl_ + (i - 8) * 25600 + nt * 32; wr[s_ % WOD] = tr2(o, o + (i < 8 ? 1280 : 3200)); };
#pragma unroll
        for (int s_ = 0; s_ < WOD; ++s_) wo_frag(s_);
        SB();
        wait_flags(0, 4 * par, 4);
        TS(13);
        { const u32 ob = WS_O + grp * 81920 + mp * 20480 + par * 8192, ot = WS_O + grp * 81920 + mp * 20480 + 16384 + par * 2048;
#pragma unroll
          for (int i = 0; i < 10; ++i) { if (i < 8) AC16(xf[i], l16, ob + (i & 4) * 1024, (i & 3) * 1024); else AC16(xf[i], l16, ot, (i - 8) * 1024); } }
#pragma unroll
        for (int s_ = 0; s_ < 50; ++s_) { const int i = s_ / 5, nt = s_ % 5;
            if (nt == 0) WAITV(9 - i, "+v"(xf[i]));
            SB(); acc[nt] = mfma16(wr[s_ % WOD], xf[i], acc[nt]); SB();
            if (s_ + WOD < 50) wo_frag(s_ + WOD); }
        SB(); TIE("+v"(xres[0]), "+v"(xres[1]), "+v"(xres[2]), "+v"(b1v)); SB();
        const u32 rb = L_WRED + w * 3072 + l16;
        STF4(rb, par ? acc[0] : acc[2]); STF4(rb + 1024, par ? acc[1] : acc[3]); STF4(rb + 2048, acc[4]);
        f32x4 ga[3], g2[3]; v2u g2p[3], sbp[3];
#pragma unroll
        for (int i = 0; i < 3; ++i) { const u32 co = (i < 2 ? par * 128 + i * 64 : 256) + g * 16;
            ga[i] = LDF4(L_OWN + co); g2[i] = LDF4(L_OWN + 320 + co); g2p[i] = LD8(L_OWN + 960 + co / 2); sbp[i] = LD8(L_OWN + 1120 + co / 2); }
        u32 xb = WS_XG + grp * 81920 + (mp >> 1) * 40960 + (16 * (mp & 1) + l15 + 32 * (g & 1)) * 16 + 5 * j * 1024, xb0 = xb + (2 * par + (g >> 1)) * 1024, xb1 = xb + 4096 + 8 * (g >> 1);
        u32 sto = WS_ST + (grp * 8 + j) * 4096 + (par * 3 + g) * 512 + (16 * mp + l15) * 8;
        f32x4 r[3]; r[0] = par ? acc[2] : acc[0]; r[1] = par ? acc[3] : acc[1]; r[2] = acc[4];
        TIE("+v"(xb0), "+v"(xb1), "+v"(sto), "+v"(r[0]), "+v"(r[1]));
        bar();
        TS(22);
        const u32 pb = L_WRED + (w ^ 1) * 3072 + l16;
        r[0] += LDF4(pb); r[1] += LDF4(pb + 1024); r[2] += LDF4(pb + 2048);
        u32 xg[6];
#pragma unroll
        for (int i = 0; i < 3; ++i) {
            x1p[2 * i] = pk2(r[i][0] * ga[i][0] + bflo(xres[i].x), r[i][1] * ga[i][1] + bfhi(xres[i].x)); x1p[2 * i + 1] = pk2(r[i][2] * ga[i][2] + bflo(xres[i].y), r[i][3] * ga[i][3] + bfhi(xres[i].y));
            xg[2 * i] = pk2(bflo(x1p[2 * i]) * g2[i][0], bfhi(x1p[2 * i]) * g2[i][1]); xg[2 * i + 1] = pk2(bflo(x1p[2 * i + 1]) * g2[i][2], bfhi(x1p[2 * i + 1]) * g2[i][3]);
        }
        CST16(xb0, ((v4u){xg[0], xg[1], xg[2], xg[3]}));
        if (hi == par) CST8(xb1, ((v2u){xg[4], xg[5]}));
        float sm[6] = {};
#pragma unroll
        for (int i = 0; i < 6; ++i) { const bool off = i >= 4 && hi != par; const u32 xa = off ? 0u : x1p[i], ya = off ? 0u : xg[i];
            sm[0] = dot2(xa, ONE2, sm[0]); sm[1] = dot2(xa, xa, sm[1]); sm[2] = dot2(ya, ONE2, sm[2]); sm[3] = dot2(ya, ya, sm[3]);
            sm[4] = dot2(ya, g2p[i >> 1][i & 1], sm[4]); sm[5] = dot2(ya, sbp[i >> 1][i & 1], sm[5]); }
        const float e0 = t32(sm[0], sm[4]), e1 = t32(sm[1], sm[5]), e2 = t32(sm[2], sm[2]), e3 = t32(sm[3], sm[3]);
        const float f0 = t16(e0, e2), f1 = t16(e1, e3);
        if (g < 3) CST8(sto, ((v2u){__float_as_uint(f0), __float_as_uint(f1)}));
        SB();
#pragma unroll
        for (int k = 32; k < 40; ++k) AC16(w1f[k], l16, w1o + (k & ~3) * 1024, (k & 3) * 1024);
        WAITV(8);
        if ((lane & 7) == 0) DMA4(L_DUMMY, xb0, a.ws);
        if ((lane & 7) == 0) DMA4(L_DUMMY, (lane & 8) && g < 3 ? sto : (xb1 & ~255u) + hi * 128, a.ws);
    }
    bar();
    if (t == 0) __hip_atomic_store((GAS u32*)(ws + gfl + 32768 + j * 128), tag, RLX_AGENT);
    TS(14);
    v4u wex;
    {
        ua_part(IC<UA1>{}, IC<32>{});
        WAITV(2 * TOUCH_OWN, W1T(32));
        ua_part(IC<32>{}, IC<40>{});
        {
            const u32 h0 = pk2(up[0], up[1]), h2 = pk2(up[2], 0.f);
            const u32 l0 = pk2(up[0] - bflo(h0), up[1] - bfhi(h0)), l2 = pk2(up[2] - bflo(h2), 0.f);
            wex.x = hi ? (b1v | (b1v << 16)) : ((h0 & 0xffffu) | (l0 << 16)); wex.y = hi ? 0u : ((h0 >> 16) | (l0 & 0xffff0000u));
            wex.z = hi ? 0u : ((h2 & 0xffffu) | (l2 << 16)); wex.w = hi ? 0u : (h2 & 0xffffu);
            TIE("+v"(wex));
        }
    }
    float cst[5];
    if (w == 7) {
#pragma unroll
        for (int i = 0; i < 5; ++i) cst[i] = 0;
#pragma unroll
        for (int r = 0; r < 2; ++r) { const int ch = lane + 64 * r; v4u gv = LD16(L_AUX + min(ch, 79) * 16), sv = LD16(L_AUX + 1280 + min(ch, 79) * 16);
#pragma unroll
            for (int e = 0; e < 4; ++e) { const u32 g_ = ch < 80 ? gv[e] : 0u, s_ = ch < 80 ? sv[e] : 0u;
                cst[0] = dot2(g_, ONE2, cst[0]); cst[1] = dot2(g_, g_, cst[1]); cst[2] = dot2(g_, s_, cst[2]); cst[3] = dot2(s_, ONE2, cst[3]); cst[4] = dot2(s_, s_, cst[4]); } }
#pragma unroll
        for (int i = 0; i < 5; ++i) { cst[i] = sum16(cst[i]); cst[i] = t16(cst[i], cst[i]); cst[i] = t32(cst[i], cst[i]); }
    }
    wait_flags(1, w, 1);
    TS(15);
    {
        v4u xi[13]; const u32 xb = WS_XG + grp * 81920 + 5 * w * 1024, sbo = WS_ST + (grp * 8 + w) * 4096;
#pragma unroll
        for (int i = 0; i < 3; ++i) AC16(xi[10 + i], l16, sbo, i * 1024);
#pragma unroll
        for (int i = 0; i < 10; ++i) AC16(xi[i], l16, xb + (i / 5) * 40960 + (i % 5 == 4 ? 4096 : 0), (i % 5 & 3) * 1024);
        WAITV(10, "+v"(xi[10]), "+v"(xi[11]), "+v"(xi[12]));
#pragma unroll
        for (int i = 0; i < 3; ++i) ST16(L_STP + w * 3072 + i * 1024 + l16, xi[10 + i]);
#pragma unroll
        for (int i = 0; i < 10; ++i) { WAITV(9 - i, "+v"(xi[i])); ST16(L_X + (i / 5) * 40960 + (5 * w + i % 5) * 1024 + l16, xi[i]); }
    }
    bar();
    TS(16);
    const int ks0 = w < 6 ? 7 * w : 42 + 6 * (w - 6), nks = w < 6 ? 7 : 6;
    v2u b2v[3];
    {
        const u32 w2o = WS_W2F + (5 * j * 54 + ks0) * 1024, w2l = WS_W2F + (5 * j * 54 + ks0 + nks - 1) * 1024;
        auto w2_load = [&](int k) { const int i = k / 5, nt = k % 5; if (i < 6) AC16R(w1f[k], l16, w2o + nt * 55296 + (i & ~3) * 1024, (i & 3) * 1024); else AC16R(w1f[k], l16, w2l + nt * 55296, 0); };
        f32x16 a0 = {}, a1 = {};
        if (w == 7) {
            __builtin_amdgcn_s_setprio(3);
            f32x2 us[3] = {};
#pragma unroll
            for (int i = 0; i < 48; ++i) us[i % 3] += *(const LAS f32x2*)(lds + L_STP + i * 512 + lane * 8);
            const f32x4 u0 = {us[0].x, us[0].y, us[1].x, us[1].y}; const f32x2 u1 = us[2];
            const float in = 1.f / 640.f, m1 = u0.x * in, m1b = bfr(m1), s1 = ln_s(fmaxf(u0.y * in - m1 * m1, 0.f)), r1 = 1.f / s1;
            const float m2 = (r1 * (u0.z - m1b * cst[0]) + cst[3]) * in;
            const float q2 = (r1 * r1 * (u0.w - 2.f * m1b * u1.x + m1b * m1b * cst[1]) + 2.f * r1 * (u1.y - m1b * cst[2]) + cst[4]) * in;
            const float s2 = ln_s(fmaxf(q2 - m2 * m2, 0.f)), bw = -bfr(m2) * s1, gm = s1 * s2;
            const u32 bh = pk2(bw, gm), bl = pk2(bw - bflo(bh), gm - bfhi(bh));
            const u32 xe = L_XE + hi * 1024 + l31 * 16;
            ST16(xe, ((v4u){pk2(-m1b, -m1b), pk2(s1, s1), (bh & 0xffffu) | (bh << 16), bl & 0xffffu}));
            ST16(xe + 512, ((v4u){(bh >> 16) | (bl & 0xffff0000u), 0u, 0u, 0u}));
            STF(L_A + lane * 4, 1.f / gm);
            asm volatile("" ::: "memory"); STF(L_MRED, 1.f);
            __builtin_amdgcn_s_setprio(0);
        }
        {
            u32 xa = L_X + l16, xb2 = L_X + 40960 + l16; TIE("+v"(xb2));
            v4u x0[3], x1[3];
#pragma unroll
            for (int ks = 0; ks < 2; ++ks) { x0[ks] = LD16(xa + ks * 1024); x1[ks] = LD16(xb2 + ks * 1024); }
#pragma unroll
            for (int ks = 0; ks < 40; ++ks) {
                if (ks + 2 < 40) { x0[(ks + 2) % 3] = LD16(xa + (ks + 2) * 1024); x1[(ks + 2) % 3] = LD16(xb2 + (ks + 2) * 1024); }
                SB();
                if (has_tile) { a0 = mfma32(w1f[ks], x0[ks % 3], a0); a1 = mfma32(w1f[ks], x1[ks % 3], a1); }
                SB();
                if (ks >= 1 && ks < 36) w2_load(ks - 1); }
        }
        { const u32 bo = (80 * j + par * 32 + g * 4) * 2; AG8(b2v[0], bo, a.b2, 0); AG8(b2v[1], bo, a.b2, 32); AG8(b2v[2], (80 * j + 64 + g * 4) * 2, a.b2, 0); }
        TS(17);
        u32 hb = WS_H + grp * 221184 + ((l31 >> 4) * 54 + nt0 + min(w, ntile - 1)) * 1024 + (l15 + 16 * hi) * 16;
        if (has_tile) {
            for (u32 sp = 0; sp < SPIN_CAP; ++sp) { if (*(volatile LAS float*)(lds + L_MRED) == 1.f) break; __builtin_amdgcn_s_sleep(1); }
            asm volatile("" ::: "memory");
            a0 = mfma32(wex, LD16(L_XE + l16), a0); a1 = mfma32(wex, LD16(L_XE + 1024 + l16), a1);
            const float A0 = LDF(L_A + l31 * 4), A1 = LDF(L_A + 128 + l31 * 4);
            const float k1 = -2.f * 0.7978845608028654f * 1.4426950408889634f, k3 = k1 * 0.044715f;
            auto gelu = [&](float z) { return z * __builtin_amdgcn_rcpf(1.f + __builtin_amdgcn_exp2f(z * (k1 + k3 * z * z))); };
#pragma unroll
            for (int tt = 0; tt < 2; ++tt)
#pragma unroll
                for (int hf = 0; hf < 2; ++hf) { v4u o; const float A = tt ? A1 : A0;
#pragma unroll
                    for (int e = 0; e < 4; ++e) { const int r = 8 * hf + 2 * e; o[e] = pk2(gelu(A * (tt ? a1[r] : a0[r])), gelu(A * (tt ? a1[r + 1] : a0[r + 1]))); }
                    CST16(hb + tt * 108 * 1024 + hf * 512, o); }
        }
        WAITV(0, W1T(0), W1T(8), "+v"(b2v[0]), "+v"(b2v[1]), "+v"(b2v[2])); TIE(W1T(16), W1T(24), "+v"(w1f[32]), "+v"(w1f[33]), "+v"(w1f[34]));
        if (has_tile && (lane & 7) < 4) DMA4(L_DUMMY, (hb & ~127u) + (lane & 1) * 512 + (lane & 2) * 54 * 1024, a.ws);
    }
    bar();
    if (t == 0) __hip_atomic_store((GAS u32*)(ws + gfl + 65536 + j * 128), tag, RLX_AGENT);
    TS(18);
    wait_flags(2, w, 1);
    TS(19);
    {
        f32x4 own[3]; v4u hf[14];
        const u32 hb = WS_H + grp * 221184 + ks0 * 1024;
        auto h_load = [&](int m, int i) { const u32 o = hb + ((mp + 1 + m) & 3) * 55296; v4u& d = hf[(m & 1) * 7 + i];
            if (i < 6) AC16(d, l16, o + (i & 4) * 1024, (i & 3) * 1024); else AC16(d, l16, o + (nks - 1) * 1024, 0); };
#pragma unroll
        for (int k = 0; k < 14; ++k) h_load(k / 7, k % 7);
#pragma unroll
        for (int m = 0; m < 4; ++m) { const int mt = (mp + 1 + m) & 3;
            f32x4 acc[5] = {};
#pragma unroll
            for (int i = 0; i < 7; ++i) { v4u& d = hf[(m & 1) * 7 + i];
                WAITV(m < 2 ? 13 : 27 - 7 * m - i, "+v"(d));
                if (i < 6 || nks == 7) {
#pragma unroll
                    for (int nt = 0; nt < 5; ++nt) acc[nt] = mfma16(w1f[i * 5 + nt], d, acc[nt]); }
                SB();
                if (m < 2) h_load(m + 2, i); }
            if (m == 3) {
                const u32 pb = L_PART + mt * 35840 + 6 * 5120 + l16;
                STF4(pb + (par ? 0 : 2048), par ? acc[0] : acc[2]); STF4(pb + (par ? 1024 : 3072), par ? acc[1] : acc[3]);
                if (hi != par) STF4(pb + 4096, acc[4]);
                own[0] = par ? acc[2] : acc[0]; own[1] = par ? acc[3] : acc[1]; own[2] = acc[4];
            } else {
                const u32 pb = L_PART + mt * 35840 + ((w - 2 * mt - 2) & 7) * 5120 + l16;
#pragma unroll
                for (int nt = 0; nt < 5; ++nt) STF4(pb + nt * 1024, acc[nt]);
            }
        }
        f32x4 gb[3], pr[3][7];
#pragma unroll
        for (int i = 0; i < 3; ++i) gb[i] = LDF4(L_OWN + 640 + ((i < 2 ? par * 32 + i * 16 : 64) + g * 4) * 4);
        bar();
        TS(20);
#pragma unroll
        for (int i = 0; i < 3; ++i)
#pragma unroll
            for (int k = 0; k < 7; ++k) pr[i][k] = LDF4(L_PART + mp * 35840 + (i < 2 ? 2 * par + i : 4) * 1024 + l16 + k * 5120);
        SB();
        GAS char* op = (GAS char*)a.out + (size_t)(row0 + 16 * mp + l15) * 1280 + j * 160 + g * 8;
#pragma unroll
        for (int i = 0; i < 3; ++i) {
            const f32x4 s = ((own[i] + pr[i][0]) + (pr[i][1] + pr[i][2])) + ((pr[i][3] + pr[i][4]) + (pr[i][5] + pr[i][6])); const v2u bb = b2v[i];
            const float o0 = bflo(x1p[2 * i]) + (s.x + bflo(bb.x)) * gb[i].x, o1 = bfhi(x1p[2 * i]) + (s.y + bfhi(bb.x)) * gb[i].y;
            const float o2 = bflo(x1p[2 * i + 1]) + (s.z + bflo(bb.y)) * gb[i].z, o3 = bfhi(x1p[2 * i + 1]) + (s.w + bfhi(bb.y)) * gb[i].w;
            if (i < 2 || hi == par) *(GAS v2u*)(op + (i < 2 ? par * 64 + i * 32 : 128)) = (v2u){pk2(o0, o1), pk2(o2, o3)};
        }
    }
    }
    TS(21); TC(31);
    asm volatile(".Lfb_code_b:");
}

extern "C" void kernel_launch(void* const* d_in, const int* in_sizes, int n_in, void* d_out, int out_size, void* d_ws, size_t ws_size, hipStream_t stream) {
    Args a;
    a.x = (const char*)d_in[0]; a.wq = (const char*)d_in[1]; a.wk = (const char*)d_in[2]; a.wv = (const char*)d_in[3]; a.wo = (const char*)d_in[4];
    a.w1 = (const char*)d_in[6]; a.b1 = (const char*)d_in[7]; a.w2 = (const char*)d_in[8]; a.b2 = (const char*)d_in[9]; a.c = (const char*)d_in[10]; a.wmod = (const char*)d_in[11];
    a.out = (char*)d_out; a.ws = (char*)d_ws;
    (void)hipFuncSetAttribute((const void*)fused_block, hipFuncAttributeMaxDynamicSharedMemorySize, L_TOTAL);
    hipLaunchKernelGGL(fused_block, dim3(256), dim3(512), L_TOTAL, stream, a);
}
```

```cpp
#include <hip/hip_runtime.h>
#include <hip/hip_bf16.h>
typedef unsigned u32; typedef unsigned long long u64; typedef unsigned short u16;
typedef u32 v4u __attribute__((ext_vector_type(4)));
typedef u32 v2u __attribute__((ext_vector_type(2)));
typedef float f32x2 __attribute__((ext_vector_type(2)));
typedef float f32x4 __attribute__((ext_vector_type(4)));
typedef float f32x16 __attribute__((ext_vector_type(16)));
typedef __bf16 bf16x8 __attribute__((ext_vector_type(8)));
typedef __bf16 bf16x2 __attribute__((ext_vector_type(2)));
typedef short s16x4 __attribute__((ext_vector_type(4)));
#define GAS __attribute__((address_space(1)))
#define LAS __attribute__((address_space(3)))
#define RLX_AGENT __ATOMIC_RELAXED, __HIP_MEMORY_SCOPE_AGENT
#define DI __device__ __forceinline__
#define SB() __builtin_amdgcn_sched_barrier(0)

constexpr u32 KVC = 12288;
constexpr u32 WS_E = 0, WS_MODG = 1024, WS_KVF = 16384, WS_WF = 18432, WS_GF = 32768, WS_KV = 163840, WS_O = WS_KV + 16 * KVC,
              WS_XG = WS_O + 32 * 81920, WS_ST = WS_XG + 32 * 81920, WS_H = WS_ST + 32 * 8 * 64 * 64, WS_W1F = WS_H + 32 * 221184,
              WS_W2F = WS_W1F + 2160 * 1024, WS_END = WS_W2F + 2160 * 1024, WS_DBG = 20u << 20;
constexpr u32 L_H = 0, L_WQ = 82944, L_WKV = 0, L_XK = 25600, L_PP = 139264, L_WO = 0, L_RING = 102400, L_WRED = 102400, L_X = 0, L_XE = 81920, L_STP = 126976, L_PART = 0,
              L_SA = 152064, L_G1 = 154624, L_AUX = 157184, L_MRED = 161024, L_A = 161536, L_OWN = 161792, L_DUMMY = 163328, L_TOTAL = 163840;
constexpr int HS = 1296;
constexpr int NPRE = 13;
#define QD 6
#define WD 6
#define KVF_I 6
#define POLLW 3
#define PSLEEP 4
#define TOUCH_OWN 1
#define UA1 24
#define WOD 8
#define MLD 0
constexpr int WPC = 7;
constexpr u32 SPIN_CAP = 400000, ONE2 = 0x3F803F80u;
template <int N> struct IC { static constexpr int value = N; };

DI float bflo(u32 v) { return __uint_as_float(v << 16); }
DI float bfhi(u32 v) { return __uint_as_float(v & 0xffff0000u); }
DI u32 pk2(float a, float b) { f32x2 v = {a, b}; return __builtin_bit_cast(u32, __builtin_convertvector(v, bf16x2)); }
DI float bfr(float a) { return bflo(pk2(a, a)); }
DI f32x16 mfma32(v4u a, v4u b, f32x16 c) { return __builtin_amdgcn_mfma_f32_32x32x16_bf16(__builtin_bit_cast(bf16x8, a), __builtin_bit_cast(bf16x8, b), c, 0, 0, 0); }
DI f32x4 mfma16(v4u a, v4u b, f32x4 c) { return __builtin_amdgcn_mfma_f32_16x16x32_bf16(__builtin_bit_cast(bf16x8, a), __builtin_bit_cast(bf16x8, b), c, 0, 0, 0); }
DI void bar() { asm volatile("s_waitcnt lgkmcnt(0)" ::: "memory"); __builtin_amdgcn_s_barrier(); asm volatile("" ::: "memory"); }
DI void vm0() { asm volatile("s_waitcnt vmcnt(0)" ::: "memory"); }
template <int CTRL> DI u32 dppu(u32 v) { return (u32)__builtin_amdgcn_update_dpp(0, (int)v, CTRL, 0xf, 0xf, true); }
template <int CTRL> DI float dpp(float v) { return __uint_as_float(dppu<CTRL>(__float_as_uint(v))); }
constexpr int X1 = 0xB1, X2 = 0x4E, HM = 0x141, R4 = 0x124, R8 = 0x128;
DI float t16(float a, float b) { auto r = __builtin_amdgcn_permlane16_swap(__float_as_uint(a), __float_as_uint(b), false, false); return __uint_as_float(r[0]) + __uint_as_float(r[1]); }
DI float t32(float a, float b) { auto r = __builtin_amdgcn_permlane32_swap(__float_as_uint(a), __float_as_uint(b), false, false); return __uint_as_float(r[0]) + __uint_as_float(r[1]); }
DI float hi32(float a) { auto r = __builtin_amdgcn_permlane32_swap(__float_as_uint(a), __float_as_uint(a), false, false); return __uint_as_float(r[1]); }
DI float sum16(float v) { v += dpp<X1>(v); v += dpp<X2>(v); v += dpp<R4>(v); v += dpp<R8>(v); return v; }
DI u32 umax16(u32 v) { v = max(v, dppu<X1>(v)); v = max(v, dppu<X2>(v)); v = max(v, dppu<R4>(v)); v = max(v, dppu<R8>(v)); return v; }
DI u32 umax64(u32 v) { v = umax16(v); auto r = __builtin_amdgcn_permlane16_swap(v, v, false, false); v = max(r[0], r[1]); auto q = __builtin_amdgcn_permlane32_swap(v, v, false, false); return max(q[0], q[1]); }

DI float dot2(u32 a, u32 b, float c) { return __builtin_amdgcn_fdot2_f32_bf16(__builtin_bit_cast(bf16x2, a), __builtin_bit_cast(bf16x2, b), c, false); }
DI u32 rfl(u32 v) { return (u32)__builtin_amdgcn_readfirstlane((int)v); }
DI float rdl(float v, int l) { return __uint_as_float((u32)__builtin_amdgcn_readlane((int)__float_as_uint(v), l)); }
DI float ln_s(float var) { return bfr(sqrtf(bfr(bfr(var) + 1e-5f))); }

struct Args { const char *x, *wq, *wk, *wv, *wo, *w1, *b1, *w2, *b2, *c, *wmod; char* out; char* ws; };

__global__ __launch_bounds__(512, 2) void fused_block(Args a) {
    extern __shared__ __attribute__((aligned(16))) char lds_[];
    LAS char* lds = (LAS char*)lds_;
    const u32 ldsb = (u32)(size_t)lds;
    const int t = threadIdx.x, lane = t & 63, w = __builtin_amdgcn_readfirstlane(t >> 6);
    const int b = blockIdx.x, xcd = b & 7, j = (b >> 3) & 7, grp = xcd * 4 + (b >> 6), row0 = grp * 64, mi = b >> 3;
    const int hi = lane >> 5, g = lane >> 4, l15 = lane & 15, l31 = lane & 31, q4 = (lane & 15) >> 2, p4 = lane & 3;
    GAS char* ws = (GAS char*)a.ws;
    const __amdgpu_buffer_rsrc_t rs = __builtin_amdgcn_make_buffer_rsrc(a.ws, 0, WS_END, 0x00020000);
#define LD16(off) (*(const LAS v4u*)(lds + (off)))
#define LD8(off) (*(const LAS v2u*)(lds + (off)))
#define LDF4(off) (*(const LAS f32x4*)(lds + (off)))
#define ST16(off, v) (*(LAS v4u*)(lds + (off)) = (v))
#define ST8(off, v) (*(LAS v2u*)(lds + (off)) = (v))
#define STF4(off, v) (*(LAS f32x4*)(lds + (off)) = (v))
#define STF(off, v) (*(LAS float*)(lds + (off)) = (v))
#define LDF(off) (*(const LAS float*)(lds + (off)))
#define TR(off) __builtin_bit_cast(v2u, __builtin_amdgcn_ds_read_tr16_b64_v4i16((LAS s16x4*)(lds + (off))))
#define GL8(p) (*(const GAS v2u*)(p))
#define CLD16(off) __builtin_amdgcn_raw_buffer_load_b128(rs, (off), 0, 16)
#define CST16(off, v) __builtin_amdgcn_raw_buffer_store_b128((v), rs, (off), 0, 16)
#define CST8(off, v) __builtin_amdgcn_raw_buffer_store_b64((v), rs, (off), 0, 16)
#define AG16(d, vo, sb, imm) asm volatile("global_load_dwordx4 %0, %1, %2 offset:%3" : "=v"(d) : "v"(vo), "s"(sb), "n"(imm))
#define AG8(d, vo, sb, imm) asm volatile("global_load_dwordx2 %0, %1, %2 offset:%3" : "=v"(d) : "v"(vo), "s"(sb), "n"(imm))
#define AG2(d, vo, sb, imm) asm volatile("global_load_ushort %0, %1, %2 offset:%3" : "=v"(d) : "v"(vo), "s"(sb), "n"(imm))
#define AC16(d, vo, so, imm) asm volatile("buffer_load_dwordx4 %0, %1, %2, %3 offen offset:%4 sc1" : "=v"(d) : "v"(vo), "s"(rs), "s"(so), "n"(imm))
#define AC16R(d, vo, so, imm) asm volatile("buffer_load_dwordx4 %0, %1, %2, %3 offen offset:%4 sc1" : "+v"(d) : "v"(vo), "s"(rs), "s"(so), "n"(imm))
#define AC8(d, vo, so, imm) asm volatile("buffer_load_dwordx2 %0, %1, %2, %3 offen offset:%4 sc1" : "=v"(d) : "v"(vo), "s"(rs), "s"(so), "n"(imm))
#define DMA16(dst, vo, sb) asm volatile("s_mov_b32 m0, %0\n\ts_nop 0\n\tglobal_load_lds_dwordx4 %1, %2" :: "s"(ldsb + (dst)), "v"(vo), "s"(sb) : "m0", "memory")
#define DMA4(dst, vo, sb) asm volatile("s_mov_b32 m0, %0\n\ts_nop 0\n\tglobal_load_lds_dword %1, %2" :: "s"(ldsb + (dst)), "v"(vo), "s"(sb) : "m0", "memory")
#define DMA16I(sl, il, vo, ig, sb) do { u32 tv_; asm volatile("s_add_u32 m0, %1, %2\n\tv_add_u32 %0, %4, %3\n\tglobal_load_lds_dwordx4 %0, %5" : "=&v"(tv_) : "s"(sl), "n"(il), "v"(vo), "n"(ig), "s"(sb) : "m0", "scc", "memory"); } while (0)
#define WAITV(n, ...) asm volatile("s_waitcnt vmcnt(%c[cnt])" : __VA_ARGS__ : [cnt] "n"(n) : "memory")
#define TIE(...) asm volatile("" : __VA_ARGS__)
#define TS(i)
#define TC(i)
    auto tr2 = [&](u32 o0, u32 o1) { v2u x0 = TR(o0), x1 = TR(o1); return (v4u){x0.x, x0.y, x1.x, x1.y}; };
    TS(0); TC(30);
    if (mi >= 28) {
        u64 pc; u32 len; asm volatile("s_getpc_b64 %0\n.Lfb_code_a:\n\ts_mov_b32 %1, .Lfb_code_b-.Lfb_code_a" : "=s"(pc), "=s"(len));
        const u32 vo = min((u32)((mi - 28) * 512 + t) * 64u, len & ~3u);
        DMA4(L_DUMMY, vo, pc);
    }
    u32 Ev; { const u32 z = 0; asm volatile("global_load_dword %0, %1, %2 sc1" : "=v"(Ev) : "v"(z), "s"(a.ws)); }
    const int m3 = mi / 3, mr3 = mi - 3 * m3; const bool crit = mr3 == 0 && mi < 30, defer = mr3 != 0 && mi < 30;
    const int mcol = mr3 == 0 ? xcd * 160 + min(m3, 9) * 16 : 1280 + xcd * 320 + (min(m3, 9) * 2 + mr3 - 1) * 16;
    auto mod_load = [&](int col, v4u (&wm)[3], u32 (&cc)[3], int part) {
        const u32 mr = t >> 1, r2 = min(mr + 512u, 639u), co = col * 2 + (t & 1) * 16;
        if (part <= 0) { AG2(cc[0], mr * 2, a.c, 0); AG2(cc[1], mr * 2, a.c, 512); AG2(cc[2], r2 * 2, a.c, 0); AG16(wm[0], mr * 7680 + co, a.wmod, 0); }
        if (part < 0 || part == 1) AG16(wm[1], (mr + 256) * 7680 + co, a.wmod, 0);
        if (part < 0 || part == 2) AG16(wm[2], r2 * 7680 + co, a.wmod, 0);
    };
    auto mod_partial = [&](v4u (&wm)[3], u32 (&cc)[3]) {
        float acc[8] = {};
#pragma unroll
        for (int i = 0; i < 3; ++i) {
            const float cv = bflo(cc[i]); u32 sl = pk2(cv * __builtin_amdgcn_rcpf(1.f + __builtin_amdgcn_exp2f(cv * -1.4426950408889634f)), 0.f);
            if (i == 2 && t >= 256) sl = 0u;
            const u32 sh = sl << 16;
#pragma unroll
            for (int e = 0; e < 4; ++e) { acc[2 * e] = dot2(wm[i][e], sl, acc[2 * e]); acc[2 * e + 1] = dot2(wm[i][e], sh, acc[2 * e + 1]); }
        }
#pragma unroll
        for (int i = 0; i < 4; ++i) acc[i] = t32(acc[i], acc[i + 4]);
#pragma unroll
        for (int i = 0; i < 2; ++i) { acc[i] = t16(acc[i], acc[i + 2]); acc[i] += dpp<X2>(acc[i]); acc[i] += dpp<R4>(acc[i]); acc[i] += dpp<R8>(acc[i]); }
        if ((lane & 14) == 0) *(LAS f32x2*)(lds + L_MRED + (w * 16 + 8 * (lane & 1) + 4 * hi + 2 * (g & 1)) * 4) = (f32x2){acc[0], acc[1]};
    };
    v4u wm[3], xr[10]; u32 cc[3];
    const int tq = j >> 1, isV = j & 1; float kn2 = 0;
    const u32 l16 = lane * 16;
    if (crit) { mod_load(mcol, wm, cc, -1); __builtin_amdgcn_s_barrier(); }
    { const u32 vo = (row0 + 8 * w) * 1280 + l16, vt = (row0 + 8 * w + 2 * g) * 1280 + 1024 + l15 * 16;
#pragma unroll
      for (int i = 0; i < 8; ++i) AG16(xr[i], vo + (i / 3) * 3840, a.x, (i % 3) * 1280);
      AG16(xr[8], vt, a.x, 0); AG16(xr[9], vt, a.x, 1280); }
    const u32 slice_vo = ((u32)lane / 10u) * 1120 + l16 + j * 160 + w * 7680, slice_sl = ldsb + w * 960;
#define SLICE(W, dst, k) do { SB(); if ((k) < 13) DMA16I(slice_sl, (dst) + (k) * 7680, slice_vo, (k) * 61440, W); \
                              else if (w < 2 || (w == 2 && lane < 40)) DMA16I(slice_sl, (dst) + (k) * 7680, slice_vo, (k) * 61440, W); SB(); } while (0)
    auto mod_publish = [&](int col, u32 tg) {
        if (lane < 8) { float s0 = 0, s1 = 0;
#pragma unroll
            for (int i = 0; i < 8; ++i) { const f32x2 v = *(const LAS f32x2*)(lds + L_MRED + (i * 16 + 2 * lane) * 4); s0 += v.x; s1 += v.y; }
            CST8(WS_MODG + (col / 2 + lane) * 8, ((v2u){pk2(s0, s1), tg})); } };
    TS(23);
    if (crit) { WAITV(10, "+v"(Ev), "+v"(wm[0]), "+v"(wm[1]), "+v"(wm[2]), "+v"(cc[0]), "+v"(cc[1]), "+v"(cc[2])); mod_partial(wm, cc); bar(); if (w == 0) mod_publish(mcol, rfl(Ev) * 1664525u + 1013904223u); }
    TS(1);
    SLICE(a.wq, L_WQ, 0); SLICE(a.wq, L_WQ, 1);
    WAITV(2);
    TIE("+v"(Ev), "+v"(xr[0]), "+v"(xr[1]), "+v"(xr[2]), "+v"(xr[3]), "+v"(xr[4]), "+v"(xr[5]), "+v"(xr[6]), "+v"(xr[7]), "+v"(xr[8]), "+v"(xr[9]));
    const u32 tag = rfl(Ev) * 1664525u + 1013904223u;
    v4u mg; const u32 mgo = min(t, 319) * 16;
    f32x2 xn[40];
    {
        float s[10], q[10];
        auto row = [&](int i) { s[i] = 0; q[i] = 0;
#pragma unroll
            for (int e = 0; e < 4; ++e) { s[i] = dot2(xr[i][e], ONE2, s[i]); q[i] = dot2(xr[i][e], xr[i][e], q[i]); } };
        row(0); row(1); SLICE(a.wq, L_WQ, 2); row(2); row(3); row(4); SLICE(a.wq, L_WQ, 3); row(5); row(6); row(7); SLICE(a.wq, L_WQ, 4); row(8); row(9);
#pragma unroll
        for (int i = 0; i < 4; ++i) { s[i] = t32(s[i], s[i + 4]); q[i] = t32(q[i], q[i + 4]); }
        SLICE(a.wq, L_WQ, 5);
        float rsd[2], nb[2];
#pragma unroll
        for (int i = 0; i < 2; ++i) { const float ss = sum16(t16(s[i], s[i + 2]) + s[8 + i]), qq = sum16(t16(q[i], q[i + 2]) + q[8 + i]);
            const float m = ss * (1.f / 640.f); rsd[i] = __builtin_amdgcn_rcpf(ln_s(fmaxf(qq * (1.f / 640.f) - m * m, 0.f))); nb[i] = -bfr(m) * rsd[i]; }
        auto nrm = [&](int i) { const float r_ = i < 8 ? rdl(rsd[i & 1], 16 * (i >> 1)) : rsd[i & 1], n_ = i < 8 ? rdl(nb[i & 1], 16 * (i >> 1)) : nb[i & 1];
#pragma unroll
            for (int e = 0; e < 4; ++e) xn[4 * i + e] = (f32x2){bflo(xr[i][e]), bfhi(xr[i][e])} * r_ + n_;
            TIE("+v"(xn[4 * i]), "+v"(xn[4 * i + 1]), "+v"(xn[4 * i + 2]), "+v"(xn[4 * i + 3])); };
        { const char* kp = isV ? a.wv : a.wk; const u32 sl = ldsb + w * 1024, vo = t * 16;
          DMA16I(sl, L_WKV, vo, 0, kp); nrm(0); DMA16I(sl, L_WKV + 8192, vo, 8192, kp); nrm(1); DMA16I(sl, L_WKV + 16384, vo, 16384, kp); if (w == 0) DMA16I(sl, L_WKV + 24576, vo, 24576, kp); }
        nrm(2); nrm(3); SLICE(a.wq, L_WQ, 6); nrm(4); nrm(5); nrm(6); SLICE(a.wq, L_WQ, 7); nrm(7); nrm(8); nrm(9); if (lane < 60) SLICE(a.wq, L_WQ, 8);
    }
    if ((w >> 1) == tq) {
#pragma unroll
        for (int i = 0; i < 10; ++i) { v4u o;
#pragma unroll
            for (int e = 0; e < 4; ++e) o[e] = pk2(xn[4 * i + e].x, xn[4 * i + e].y);
            ST16(i < 8 ? L_XK + (8 * (w & 1) + i) * HS + l16 : L_XK + (8 * (w & 1) + 2 * g + i - 8) * HS + 1024 + l15 * 16, o); } }
    TS(2);
    WAITV(3); bar();
    AC16(mg, mgo, WS_MODG, 0);
    TS(3);
    const int hh = w & 3, th = w >> 2, ks2 = min(w + 16, 19);
    v4u wf[40], kf[3][2]; f32x2 xk[12];
    { const u32 wb = L_WKV + (8 * g + q4) * 40 + p4 * 8;
#pragma unroll
      for (int i = 0; i < 3; ++i) { const u32 o = wb + (i < 2 ? w + 8 * i : ks2) * 1280;
          const v4u xv = LD16(L_XK + l15 * HS + g * 16 + (i < 2 ? w + 8 * i : ks2) * 64);
          kf[i][0] = tr2(o, o + 160); kf[i][1] = tr2(o + 32, o + 192);
#pragma unroll
          for (int e = 0; e < 4; ++e) xk[4 * i + e] = (f32x2){bflo(xv[e]), bfhi(xv[e])};
          TIE("+v"(xk[4 * i]), "+v"(xk[4 * i + 1]), "+v"(xk[4 * i + 2]), "+v"(xk[4 * i + 3])); } }
    const u32 wqb = L_WQ + (8 * hi + q4) * 160 + (20 * hh + 16 * (g & 1) + 4 * p4) * 2; u32 wq2 = wqb + 20 * 2560; TIE("+v"(wq2));
    auto wq_frag = [&](int ks) { const int k = ks < 27 ? ks : ks - 27; const u32 o = k < 20 ? wqb + k * 2560 : wq2 + (k - 20) * 2560; wf[ks] = tr2(o, o + 640); };
#pragma unroll
    for (int ks = 0; ks < NPRE; ++ks) wq_frag(ks);
    WAITV(0, "+v"(mg));
    TS(26);
    if (w < 5) {
        for (u32 sp = 0; sp < SPIN_CAP; ++sp) { if (__all(mg.y == tag && mg.w == tag)) break; AC16(mg, mgo, WS_MODG, 0); WAITV(0, "+v"(mg)); }
        f32x4 f = {bflo(mg.x), bfhi(mg.x), bflo(mg.z), bfhi(mg.z)};
        if (t >= 160) { const u32 p0 = pk2(1.f + f.x, 1.f + f.y), p1 = pk2(1.f + f.z, 1.f + f.w); f = (f32x4){bflo(p0), bfhi(p0), bflo(p1), bfhi(p1)}; }
        STF4(L_SA + t * 16, f);
    }
    bar();
    TS(4);
    {
        f32x4 acc0 = {}, acc1 = {}, gv[6], sv[6];
#pragma unroll
        for (int i = 0; i < 6; ++i) { const u32 mo = (i < 4 ? w + 8 * (i >> 1) : ks2) * 128 + g * 32 + (i & 1) * 16; gv[i] = LDF4(L_G1 + mo); sv[i] = LDF4(L_SA + mo); }
        SB();
#pragma unroll
        for (int i = 0; i < 3; ++i) { v4u hf;
#pragma unroll
            for (int e = 0; e < 4; ++e) { const f32x4 gg = gv[2 * i + (e >> 1)], ss = sv[2 * i + (e >> 1)]; const f32x2 v = xk[4 * i + e] * (e & 1 ? gg.zw : gg.xy) + (e & 1 ? ss.zw : ss.xy); hf[e] = pk2(v.x, v.y); }
            if (i == 2 && w >= 4) hf = (v4u){};
            if (isV) { acc0 = mfma16(hf, kf[i][0], acc0); acc1 = mfma16(hf, kf[i][1], acc1); } else { acc0 = mfma16(kf[i][0], hf, acc0); acc1 = mfma16(kf[i][1], hf, acc1); } }
        STF4(L_WKV + w * 1280 + l16, acc0);
        if (isV ? l15 < 4 : g == 0) STF4(L_WKV + w * 1280 + 1024 + (isV ? 4 * g + l15 : l15) * 16, acc1);
    }
    const int key0 = row0 + 16 * tq; const u32 cb = WS_KV + (key0 >> 7) * KVC, tile = (key0 >> 5) & 3, kk = (key0 & 31) + l15, vb = cb + 4096 + tile * 1536 + (tq & 1) * 768;
    u32 kvs0 = isV ? vb + (g & 1) * 384 + l15 * 16 + 8 * (g >> 1) : cb + tile * 1024 + (kk + 32 * (g & 1)) * 16 + 8 * (g >> 1);
    u32 kvs1 = isV ? vb + (q4 & 1) * 384 + (16 + 4 * g + p4) * 16 + 8 * (q4 >> 1) : cb + 10240 + tile * 512 + kk * 16, kvr = L_WKV + g * 1280 + 1024 + l15 * 16;
    TIE("+v"(kvs0), "+v"(kvs1), "+v"(kvr));
    bar();
    TS(5);
    if (w == 0) {
        __builtin_amdgcn_s_setprio(3);
        f32x4 r0[6];
#pragma unroll
        for (int k = 0; k < 6; ++k) r0[k] = LDF4(L_WKV + k * 1280 + l16);
        SB();
        r0[0] += r0[1]; r0[2] += r0[3]; r0[1] = LDF4(L_WKV + 6 * 1280 + l16); r0[3] = LDF4(L_WKV + 7 * 1280 + l16); SB();
        r0[0] += r0[4]; r0[2] += r0[5];
        f32x4 s1 = LDF4(kvr), s2 = LDF4(kvr + 5120); SB();
        const f32x4 s0 = (r0[0] + r0[1]) + (r0[2] + r0[3]);
        const v2u p0 = {pk2(s0.x, s0.y), pk2(s0.z, s0.w)};
        CST8(kvs0, p0); SB();
        s1 += s2;
        const float u0 = t32(s1.x, s1.y), u1 = t32(s1.z, s1.w), u2 = t16(u0, u1);
        const auto uy = __builtin_amdgcn_permlane32_swap(__float_as_uint(u2), __float_as_uint(u2), false, false);
        const u32 pa = pk2(__uint_as_float(uy[0]), __uint_as_float(uy[1]));
        const auto pz = __builtin_amdgcn_permlane16_swap(pa, pa, false, false);
        v2u p1 = {pz[0], pz[1]};
        if (isV) {
            if (g) { const u32 c = p4 == 0 ? ONE2 : 0u; p1 = (v2u){c, c}; }
            if (g < 2) CST8(kvs1, p1);
        } else {
            if (g == 0) CST16(kvs1, ((v4u){p1.x, p1.y, ONE2, ONE2}));
            kn2 = dot2(p0.x, p0.x, dot2(p0.y, p0.y, 0.f));
            kn2 = t16(kn2, kn2); kn2 = t32(kn2, kn2);
            kn2 = dot2(p1.x, p1.x, dot2(p1.y, p1.y, kn2));
            kn2 = __uint_as_float(umax16(__float_as_uint(kn2)));
        }
    }
    TS(6);
    {
        const u32 hb0 = L_H + 8 * w * HS + l16, ht0 = L_H + (8 * w + 2 * g) * HS + 1024 + l15 * 16;
        f32x4 gm[2], sm[2], gt[2], st[2];
#pragma unroll
        for (int k = 0; k < 2; ++k) { gm[k] = LDF4(L_G1 + lane * 32 + k * 16); sm[k] = LDF4(L_SA + lane * 32 + k * 16); }
        SLICE(a.wq, L_WQ - 69120, 9); SLICE(a.wq, L_WQ - 69120, 10);
#pragma unroll
        for (int i = 0; i < 10; ++i) { v4u o;
            if (i == 4) {
#pragma unroll
                for (int k = 0; k < 2; ++k) { gt[k] = LDF4(L_G1 + 2048 + l15 * 32 + k * 16); st[k] = LDF4(L_SA + 2048 + l15 * 32 + k * 16); }
                SLICE(a.wq, L_WQ - 69120, 12); SLICE(a.wq, L_WQ - 69120, 13); }
            if (i == 2) SLICE(a.wq, L_WQ - 69120, 11);
#pragma unroll
            for (int e = 0; e < 4; ++e) { const f32x4 gg = i < 8 ? gm[e >> 1] : gt[e >> 1], ss = i < 8 ? sm[e >> 1] : st[e >> 1];
                const f32x2 v = xn[4 * i + e] * (e & 1 ? gg.zw : gg.xy) + (e & 1 ? ss.zw : ss.xy); o[e] = pk2(v.x, v.y); }
            if (i == KVF_I && w == 0) { WAITV(5); if (lane == 0) CST8(WS_KVF + b * 8, ((v2u){__float_as_uint(kn2), tag})); __builtin_amdgcn_s_setprio(0); TS(7); }
            ST16(i >= 8 ? ht0 + (i - 8) * HS : hb0 + i * HS, o); SB(); }
    }
    if (w == 0) WAITV(1); else WAITV(0);
    bar();
    TS(24);
    v4u wm2[3]; u32 cc2[3];
    u32 ppd[3], ppv[3]; v4u ppf[3];
    const bool pp3 = w == 1 && b < 224, pp3w2 = b >= 112;
    auto pp_addr = [&](int r) { const int i = r < 2 ? b * 8 + w : 2048 + min(pp3w2 ? b - 112 : b, 111); const bool isw2 = r < 2 ? r == 1 : pp3w2;
        if (isw2) { const int ks = i / 40, nt = i - ks * 40; ppv[r] = ks * 40960 + nt * 32 + (lane >> 1) * 1280 + (lane & 1) * 16; ppd[r] = WS_W2F + (nt * 54 + ks) * 1024; }
        else { const int ks = i / 54, nt = i - ks * 54, jj = ks / 5, ii = ks - jj * 5, lr = lane >> 2;
            ppv[r] = (80 * jj + (ii < 4 ? 32 * (ii >> 1) + 8 * (ii & 1) : 64)) * 3456 + nt * 64 + (lr + (ii < 4 ? (lr & 8) : 0)) * 3456 + p4 * 16; ppd[r] = WS_W1F + (nt * 40 + ks) * 1024; }
        ppd[r] = rfl(ppd[r]); TIE("+v"(ppv[r]), "+s"(ppd[r])); };
    auto pp_load = [&](int r) { DMA16(L_PP + (r < 2 ? w + 8 * r : 16) * 1024, ppv[r], (r < 2 ? r == 1 : pp3w2) ? a.w2 : a.w1); };
    auto pp_read = [&](int r) { const bool isw2 = r < 2 ? r == 1 : pp3w2;
        const u32 o = L_PP + (r < 2 ? w + 8 * r : 16) * 1024 + (isw2 ? (16 * (g >> 1) + 4 * (g & 1) + q4) * 32 + p4 * 8 : (4 * hi + q4) * 64 + (g & 1) * 32 + p4 * 8);
        ppf[r] = tr2(o, o + (isw2 ? 256 : 512)); };
    auto pp_store = [&](int r) { __builtin_amdgcn_raw_buffer_store_b128(ppf[r], rs, l16, ppd[r], 16); };
    v4u qf0, qf1;
#define TOUCH(vo, so) asm volatile("s_mov_b32 m0, %0\n\ts_nop 0\n\tbuffer_load_dword %1, %2, %3 offen lds" :: "s"(ldsb + L_DUMMY), "v"((u32)(vo)), "s"(rs), "s"(so) : "m0", "memory")
    const u32 kvo0 = t * 16;
#define KVDMA(sl, il, sg, ig) do { u32 ts_; asm volatile("s_add_u32 m0, %1, %2\n\ts_add_u32 %0, %3, %4\n\tbuffer_load_dwordx4 %5, %6, %0 offen sc1 lds" : "=&s"(ts_) : "s"(sl), "n"(il), "s"(sg), "n"(ig), "v"(l16), "s"(rs) : "m0", "scc", "memory"); } while (0)
    {
        f32x16 acc = {}; v4u hq[QD];
        const u32 hb = L_H + (32 * th + l31) * HS + hi * 16;
#pragma unroll
        for (int ks = 0; ks < QD; ++ks) hq[ks] = LD16(hb + ks * 32);
#pragma unroll
        for (int ks = 0; ks < 40; ++ks) {
            SB(); acc = mfma32(wf[ks], hq[ks % QD], acc); SB();
            if (ks + QD < 40) hq[ks % QD] = LD16(hb + (ks + QD) * 32);
            if (ks + WD >= NPRE && ks + WD < 40) wq_frag(ks + WD);
            if (ks == 1 || ks == 3 || ks == 5) pp_addr(ks >> 1);
            if (ks == MLD && defer) mod_load(mcol, wm2, cc2, -1);
        }
        SB();
        const float sc = 0.22360679774997896f * 1.4426950408889634f;
        qf0 = (v4u){pk2(acc[0] * sc, acc[1] * sc), pk2(acc[2] * sc, acc[3] * sc), pk2(acc[4] * sc, acc[5] * sc), pk2(acc[6] * sc, acc[7] * sc)};
        qf1 = (v4u){hi ? 0u : pk2(acc[8] * sc, acc[9] * sc), hi ? 0u : pk2(acc[10] * sc, acc[11] * sc), 0u, 0u};
        float qn2 = dot2(qf0.x, qf0.x, dot2(qf0.y, qf0.y, dot2(qf0.z, qf0.z, dot2(qf0.w, qf0.w, dot2(qf1.x, qf1.x, dot2(qf1.y, qf1.y, 0.f))))));
        qn2 = t32(qn2, qn2);
        TS(8);
        if (defer) { WAITV(0, "+v"(wm2[0]), "+v"(wm2[1]), "+v"(wm2[2]), "+v"(cc2[0]), "+v"(cc2[1]), "+v"(cc2[2])); mod_partial(wm2, cc2); }
        v4u kv0[6];
        if (w < 4) {
            v4u f0, f1;
            for (u32 sp = 0; sp < SPIN_CAP; ++sp) { f0 = CLD16(WS_KVF + l16); f1 = CLD16(WS_KVF + 1024 + l16);
                if (__all(f0.y == tag && f0.w == tag && f1.y == tag && f1.w == tag)) break;
                __builtin_amdgcn_s_sleep(PSLEEP); }
#pragma unroll
            for (int i = 0; i < 6; ++i) AC16(kv0[i], kvo0, WS_KV + i * 4096, 0);
            const u32 km = umax64(max(max(f0.x, f0.z), max(f1.x, f1.z)));
            if (t == 0) STF(L_A, __uint_as_float(km));
        }
        TS(9);
        bar();
        if (w < 4) {
            WAITV(3, "+v"(kv0[0]), "+v"(kv0[1]), "+v"(kv0[2]));
#pragma unroll
            for (int i = 0; i < 3; ++i) ST16(L_RING + i * 4096 + kvo0, kv0[i]);
            WAITV(0, "+v"(kv0[3]), "+v"(kv0[4]), "+v"(kv0[5]));
#pragma unroll
            for (int i = 3; i < 6; ++i) ST16(L_RING + i * 4096 + kvo0, kv0[i]);
        }
        const float kmax2 = LDF(L_A);
        const float Msh = bfr(__builtin_amdgcn_sqrtf(qn2 * kmax2) * 1.01f + 1e-30f);
        qf1.z = hi ? 0u : (pk2(-Msh, 0.f) & 0xffffu);
        TS(25);
    }
    bar();
    if (b == 0 && t == 128) __hip_atomic_store((GAS u32*)(ws + WS_E), tag, RLX_AGENT);
    TS(10); TC(28);
    const int ntile = j < 6 ? 7 : 6, nt0 = j < 6 ? 7 * j : 42 + 6 * (j - 6); const bool has_tile = w < ntile;
    const u32 w1o = WS_W1F + (nt0 + min(w, ntile - 1)) * 40960;
    v4u w1f[40];
    f32x16 oacc = {};
    {
        const u32 ak0 = L_RING + lane * 16, ak1 = L_RING + 10240 + l31 * 16, av = L_RING + 4096 + (hi * 24 + (l31 < 24 ? l31 : (l31 < 28 ? l31 - 4 : l31 - 12))) * 16;
        v4u k0 = LD16(ak0), k1 = LD16(ak1);
        f32x16 sc = mfma32(k0, qf0, (f32x16){}); sc = mfma32(k1, qf1, sc);
        k0 = LD16(ak0 + 1024); k1 = LD16(ak1 + 512);
        v4u p1 = {}, v1 = {}, pf0, pf1;
        const u32 kvl = ldsb + L_RING + w * 1024, kvg = WS_KV + w * 1024, tw2 = WS_W2F + mi * 71680 + w * 1280;
#define EX(i) __builtin_amdgcn_exp2f(sc[i])
        const int mu = t - 320; const u32 mgo2 = (t < 320 ? 1920 + 4 * t : (mu < 20 ? 1280 + 80 * j + 4 * mu : 3200 + 80 * j + 4 * (min(mu, 39) - 20))) * 4;
        auto chunk = [&](auto CC) {
            constexpr int c = decltype(CC)::value; constexpr u32 cur = (c % 3) * KVC, nxt = ((c + 1) % 3) * KVC, fre = ((c + 2) % 3) * KVC;
            constexpr int NY = c == 2 ? 4 : (c < 7 ? 2 : (c == 7 ? 1 : 5));
#pragma unroll
            for (int i = 0; i < 4; ++i) {
                v4u p0; f32x16 sn; float e[16];
                const u32 kb = i < 2 ? cur + (i + 2) * 1024 : nxt + (i - 2) * 1024, kb1 = i < 2 ? cur + (i + 2) * 512 : nxt + (i - 2) * 512;
                sn = mfma32(k0, qf0, (f32x16){}); SB();
                const v4u v0 = LD16(cur + av + i * 1536); k0 = LD16(kb + ak0); SB();
                e[0] = EX(0); e[1] = EX(1); e[2] = EX(2); e[3] = EX(3); SB();
                oacc = mfma32(v1, p1, oacc); SB();
                v1 = LD16(cur + av + i * 1536 + 768); SB();
                e[4] = EX(4); e[5] = EX(5); SB(); p0.x = pk2(e[0], e[1]); p0.y = pk2(e[2], e[3]); SB(); e[6] = EX(6); SB();
                sn = mfma32(k1, qf1, sn); SB();
                k1 = LD16(kb1 + ak1); SB();
                e[7] = EX(7); e[8] = EX(8); SB(); p0.z = pk2(e[4], e[5]); SB(); e[9] = EX(9); SB(); p0.w = pk2(e[6], e[7]); SB(); e[10] = EX(10); e[11] = EX(11); SB();
                oacc = mfma32(v0, p0, oacc); SB();
                e[12] = EX(12); e[13] = EX(13); SB(); p1.x = pk2(e[8], e[9]); SB(); e[14] = EX(14); e[15] = EX(15); SB(); p1.y = pk2(e[10], e[11]); p1.z = pk2(e[12], e[13]); p1.w = pk2(e[14], e[15]);
                TIE("+v"(p1));
                if (i == 0) {
                    if constexpr (c == 4) { if (t == 64) CST8(WS_WF + b * 8, ((v2u){0u, tag})); }
                    if constexpr (c == 3) { if (defer && w == 7) mod_publish(mcol, tag); }
                    if constexpr (c == WPC) { if (w == 7) { AC16(pf0, l16, WS_WF, 0); AC16(pf1, l16, WS_WF, 1024); } }
                    if constexpr (c < 14) { if (w < 4) KVDMA(kvl, fre + 8192, kvg, (c + 2) * KVC + 8192); KVDMA(kvl, fre, kvg, (c + 2) * KVC); }
                    if constexpr (c == 2) { pp_read(0); pp_read(1); pp_read(2); }
                }
                if (i == 1) {
                    if constexpr (c == 2) { pp_store(0); pp_store(1); if (pp3) pp_store(2); }
                    if constexpr (c == 14) AC16(mg, mgo2, WS_MODG, 0);
                }
                if (i == 3) { if constexpr (c == WPC) { if (w == 7) {
                    WAITV(2, "+v"(pf0), "+v"(pf1));
                    for (u32 sp = 0; sp < SPIN_CAP; ++sp) { if (__all(pf0.y == tag && pf0.w == tag && pf1.y == tag && pf1.w == tag)) break;
                        __builtin_amdgcn_s_sleep(4); AC16(pf0, l16, WS_WF, 0); AC16(pf1, l16, WS_WF, 1024); WAITV(0, "+v"(pf0), "+v"(pf1)); } } } }
                if (i >= 2) {
                    if constexpr (c == 0) { pp_load(i - 2); if (i == 3 && pp3) pp_load(2); }
                    if constexpr (c >= 1 && c < 8) { if (i == 2) SLICE(a.wo, L_WO, 2 * c - 2); else SLICE(a.wo, L_WO, 2 * c - 1); }
                    if constexpr (c > WPC) { const int k = 4 * (c - WPC - 1) + 2 * (i - 2); AC16(w1f[k], l16, w1o + (k & ~3) * 1024, (k & 3) * 1024); AC16(w1f[k + 1], l16, w1o + (k & ~3) * 1024, ((k + 1) & 3) * 1024); }
                    if constexpr (c > WPC && c < WPC + 8) { if (i == 3 && lane < 10) TOUCH(l16 * 8, tw2 + (c - WPC - 1) * 10240); }
                }
                SB();
                sc = sn;
            }
            if constexpr (c < 14) WAITV(NY);
            bar();
        };
        chunk(IC<0>{}); chunk(IC<1>{}); chunk(IC<2>{}); chunk(IC<3>{}); chunk(IC<4>{}); chunk(IC<5>{}); chunk(IC<6>{}); chunk(IC<7>{});
        chunk(IC<8>{}); chunk(IC<9>{}); chunk(IC<10>{}); chunk(IC<11>{}); chunk(IC<12>{}); chunk(IC<13>{}); chunk(IC<14>{}); chunk(IC<15>{});
        oacc = mfma32(v1, p1, oacc);
    }
    TS(11); TC(29);
    {
    int t2 = threadIdx.x; TIE("+v"(t2));
    const int t = t2, lane = t & 63, hi = lane >> 5, g = lane >> 4, l15 = lane & 15, l31 = lane & 31, q4 = (lane & 15) >> 2, p4 = lane & 3; const u32 l16 = lane * 16;
    float lsum = hi32(oacc[8]);
    if (__any(!(lsum >= 1e-12f && lsum < 3e38f))) {
        qf1.z = 0u;
        const u32 ak0 = WS_KV + lane * 16, ak1 = WS_KV + 10240 + l31 * 16, av = WS_KV + 4096 + (hi * 24 + (l31 < 24 ? l31 : 20)) * 16;
        float mx = -__builtin_inff();
        for (int tI = 0; tI < 64; ++tI) { const u32 cb = (tI >> 2) * KVC, i = tI & 3;
            const v4u k0 = CLD16(cb + ak0 + i * 1024), k1 = CLD16(cb + ak1 + i * 512);
            f32x16 s = mfma32(k0, qf0, (f32x16){}); s = mfma32(k1, qf1, s);
#pragma unroll
            for (int r = 0; r < 16; ++r) mx = fmaxf(mx, s[r]); }
        mx = fmaxf(mx, __shfl_xor(mx, 32));
        oacc = (f32x16){};
        for (int tI = 0; tI < 64; ++tI) { const u32 cb = (tI >> 2) * KVC, i = tI & 3;
            const v4u k0 = CLD16(cb + ak0 + i * 1024), k1 = CLD16(cb + ak1 + i * 512);
            const v4u v0 = CLD16(cb + av + i * 1536), v1 = CLD16(cb + av + i * 1536 + 768);
            f32x16 s = mfma32(k0, qf0, (f32x16){}); s = mfma32(k1, qf1, s);
            v4u p0, p1;
#pragma unroll
            for (int e = 0; e < 4; ++e) { p0[e] = pk2(__builtin_amdgcn_exp2f(s[2 * e] - mx), __builtin_amdgcn_exp2f(s[2 * e + 1] - mx));
                                          p1[e] = pk2(__builtin_amdgcn_exp2f(s[8 + 2 * e] - mx), __builtin_amdgcn_exp2f(s[9 + 2 * e] - mx)); }
            oacc = mfma32(v0, p0, oacc); oacc = mfma32(v1, p1, oacc); }
        lsum = hi32(oacc[8]);
    }
    const u32 gfl = WS_GF + grp * 1024;
    const int mp = w >> 1, par = w & 1;
    v2u xres[3]; u32 b1v;
    {
        const float il = __builtin_amdgcn_rcpf(lsum); const int mt = 2 * th + (l31 >> 4);
        const v4u o0 = {pk2(oacc[0] * il, oacc[1] * il), pk2(oacc[2] * il, oacc[3] * il), pk2(oacc[4] * il, oacc[5] * il), pk2(oacc[6] * il, oacc[7] * il)};
        const u32 ob = WS_O + grp * 81920 + mt * 20480 + l15 * 16;
        CST16(ob + (2 * j + (hh >> 1)) * 1024 + (2 * (hh & 1) + hi) * 256, o0);
        const int ci = 64 + 2 * j + (hh >> 1);
        if (!hi) CST8(ob + ci * 256 + 8 * (hh & 1), ((v2u){pk2(oacc[8] * il, oacc[9] * il), pk2(oacc[10] * il, oacc[11] * il)}));
        SB();
        const u32 xo = (row0 + 16 * mp + l15) * 1280 + j * 160 + g * 8;
        AG8(xres[0], xo + par * 64, a.x, 0); AG8(xres[1], xo + par * 64, a.x, 32); AG8(xres[2], xo, a.x, 128);
        AG2(b1v, ((nt0 + min(w, ntile - 1)) * 32 + l31) * 2, a.b1, 0);
    }
    {
        WAITV(6, "+v"(mg));
        if (t == 511) STF(L_MRED, 0.f);
        const int mu = t - 320; const u32 mgo2 = (t < 320 ? 1920 + 4 * t : (mu < 20 ? 1280 + 80 * j + 4 * mu : 3200 + 80 * j + 4 * (min(mu, 39) - 20))) * 4;
                        if (w < 6) {
                            for (u32 sp = 0; sp < SPIN_CAP; ++sp) { if (__all(mg.y == tag && mg.w == tag)) break; AC16(mg, mgo2, WS_MODG, 0); WAITV(0, "+v"(mg)); }
                            v2u v = {mg.x, mg.z}; f32x4 f = {bflo(v.x), bfhi(v.x), bflo(v.y), bfhi(v.y)};
                            if (w < 5) {
                                const int isg = t >= 160, tp = t - 160 * isg, jj = (tp * 205) >> 12, r4 = tp - jj * 20;
                                if (isg) { v = (v2u){pk2(1.f + f.x, 1.f + f.y), pk2(1.f + f.z, 1.f + f.w)}; f = (f32x4){bflo(v.x), bfhi(v.x), bflo(v.y), bfhi(v.y)}; }
                                const int o = r4 < 16 ? (r4 >> 3) * 64 + (r4 & 3) * 16 + (r4 & 4) * 2 : 128 + (r4 & 1) * 16 + (r4 & 2) * 4;
                                ST8(L_AUX + (isg ? 0 : 1280) + jj * 160 + o, v);
                                if (jj == j) { if (isg) STF4(L_OWN + 320 + r4 * 16, f); ST8(L_OWN + (isg ? 960 : 1120) + r4 * 8, v); }
                            } else if (mu < 40) STF4(L_OWN + (mu < 20 ? mu * 16 : 640 + (mu - 20) * 16), f);
                        } else if (t < 464) ST16(L_AUX + 2560 + (t - 384) * 16, ((v4u){ONE2, ONE2, ONE2, ONE2}));
    }
#define W1T(k) "+v"(w1f[k]), "+v"(w1f[k + 1]), "+v"(w1f[k + 2]), "+v"(w1f[k + 3]), "+v"(w1f[k + 4]), "+v"(w1f[k + 5]), "+v"(w1f[k + 6]), "+v"(w1f[k + 7])
    WAITV(4, W1T(0), W1T(8)); TIE(W1T(16), W1T(24));
    if (lane < 12) DMA4(L_DUMMY, WS_O + grp * 81920 + (2 * th + (lane & 1)) * 20480 + (lane < 8 ? (2 * j + (hh >> 1)) * 1024 + (hh & 1) * 512 + (lane >> 1) * 128 : (64 + 2 * j + (hh >> 1)) * 256 + (lane & 2) * 64), a.ws);
    bar();
    if (t == 0) __hip_atomic_store((GAS u32*)(ws + gfl + j * 128), tag, RLX_AGENT);
    TS(12);
    auto wait_flags = [&](int phase, int first, int n) {
        for (u32 sp = 0; sp < SPIN_CAP; ++sp) { const u32 f = __builtin_amdgcn_raw_buffer_load_b32(rs, gfl + phase * 32768 + (first + (lane & (n - 1))) * 128, 0, 16);
            if (__all(f == tag)) break; __builtin_amdgcn_s_sleep(1); } };
    const u32 ab = L_AUX + min(l31, 2) * 1280 + hi * 16;
    float up[3];
    auto ua_part = [&](auto K0, auto K1) { constexpr int k0 = decltype(K0)::value, k1 = decltype(K1)::value; f32x16 ua = {}; v4u af[4];
#pragma unroll
        for (int ks = k0; ks < k0 + 4; ++ks) af[ks & 3] = LD16(ab + ks * 32);
#pragma unroll
        for (int ks = k0; ks < k1; ++ks) { SB(); ua = mfma32(af[ks & 3], w1f[ks], ua); SB(); if (ks + 4 < k1) af[ks & 3] = LD16(ab + (ks + 4) * 32); }
#pragma unroll
        for (int i = 0; i < 3; ++i) up[i] = k0 ? up[i] + ua[i] : ua[i];
        TIE("+v"(up[0]), "+v"(up[1]), "+v"(up[2])); };
    ua_part(IC<0>{}, IC<UA1>{});
    u32 x1p[6];
    {
        v4u xf[10], wr[WOD]; f32x4 acc[5] = {};
        const u32 wm_ = L_WO + (320 * par + 20 * (g >> 1) + 4 * (g & 1) + q4) * 160 + p4 * 8, wl_ = L_WO + (320 * par + 40 * g + 16 + q4) * 160 + p4 * 8;
        auto wo_frag = [&](int s_) { const int i = s_ / 5, nt = s_ % 5; const u32 o = i < 8 ? wm_ + i * 6400 + nt * 32 : wl_ + (i - 8) * 25600 + nt * 32; wr[s_ % WOD] = tr2(o, o + (i < 8 ? 1280 : 3200)); };
#pragma unroll
        for (int s_ = 0; s_ < WOD; ++s_) wo_frag(s_);
        SB();
        wait_flags(0, 4 * par, 4);
        TS(13);
        { const u32 ob = WS_O + grp * 81920 + mp * 20480 + par * 8192, ot = WS_O + grp * 81920 + mp * 20480 + 16384 + par * 2048;
#pragma unroll
          for (int i = 0; i < 10; ++i) { if (i < 8) AC16(xf[i], l16, ob + (i & 4) * 1024, (i & 3) * 1024); else AC16(xf[i], l16, ot, (i - 8) * 1024); } }
#pragma unroll
        for (int s_ = 0; s_ < 50; ++s_) { const int i = s_ / 5, nt = s_ % 5;
            if (nt == 0) WAITV(9 - i, "+v"(xf[i]));
            SB(); acc[nt] = mfma16(wr[s_ % WOD], xf[i], acc[nt]); SB();
            if (s_ + WOD < 50) wo_frag(s_ + WOD); }
        SB(); TIE("+v"(xres[0]), "+v"(xres[1]), "+v"(xres[2]), "+v"(b1v)); SB();
        const u32 rb = L_WRED + w * 3072 + l16;
        STF4(rb, par ? acc[0] : acc[2]); STF4(rb + 1024, par ? acc[1] : acc[3]); STF4(rb + 2048, acc[4]);
        f32x4 ga[3], g2[3]; v2u g2p[3], sbp[3];
#pragma unroll
        for (int i = 0; i < 3; ++i) { const u32 co = (i < 2 ? par * 128 + i * 64 : 256) + g * 16;
            ga[i] = LDF4(L_OWN + co); g2[i] = LDF4(L_OWN + 320 + co); g2p[i] = LD8(L_OWN + 960 + co / 2); sbp[i] = LD8(L_OWN + 1120 + co / 2); }
        u32 xb = WS_XG + grp * 81920 + (mp >> 1) * 40960 + (16 * (mp & 1) + l15 + 32 * (g & 1)) * 16 + 5 * j * 1024, xb0 = xb + (2 * par + (g >> 1)) * 1024, xb1 = xb + 4096 + 8 * (g >> 1);
        u32 sto = WS_ST + (grp * 8 + j) * 4096 + (par * 3 + g) * 512 + (16 * mp + l15) * 8;
        f32x4 r[3]; r[0] = par ? acc[2] : acc[0]; r[1] = par ? acc[3] : acc[1]; r[2] = acc[4];
        TIE("+v"(xb0), "+v"(xb1), "+v"(sto), "+v"(r[0]), "+v"(r[1]));
        bar();
        TS(22);
        const u32 pb = L_WRED + (w ^ 1) * 3072 + l16;
        r[0] += LDF4(pb); r[1] += LDF4(pb + 1024); r[2] += LDF4(pb + 2048);
        u32 xg[6];
#pragma unroll
        for (int i = 0; i < 3; ++i) {
            x1p[2 * i] = pk2(r[i][0] * ga[i][0] + bflo(xres[i].x), r[i][1] * ga[i][1] + bfhi(xres[i].x)); x1p[2 * i + 1] = pk2(r[i][2] * ga[i][2] + bflo(xres[i].y), r[i][3] * ga[i][3] + bfhi(xres[i].y));
            xg[2 * i] = pk2(bflo(x1p[2 * i]) * g2[i][0], bfhi(x1p[2 * i]) * g2[i][1]); xg[2 * i + 1] = pk2(bflo(x1p[2 * i + 1]) * g2[i][2], bfhi(x1p[2 * i + 1]) * g2[i][3]);
        }
        CST16(xb0, ((v4u){xg[0], xg[1], xg[2], xg[3]}));
        if (hi == par) CST8(xb1, ((v2u){xg[4], xg[5]}));
        float sm[6] = {};
#pragma unroll
        for (int i = 0; i < 6; ++i) { const bool off = i >= 4 && hi != par; const u32 xa = off ? 0u : x1p[i], ya = off ? 0u : xg[i];
            sm[0] = dot2(xa, ONE2, sm[0]); sm[1] = dot2(xa, xa, sm[1]); sm[2] = dot2(ya, ONE2, sm[2]); sm[3] = dot2(ya, ya, sm[3]);
            sm[4] = dot2(ya, g2p[i >> 1][i & 1], sm[4]); sm[5] = dot2(ya, sbp[i >> 1][i & 1], sm[5]); }
        const float e0 = t32(sm[0], sm[4]), e1 = t32(sm[1], sm[5]), e2 = t32(sm[2], sm[2]), e3 = t32(sm[3], sm[3]);
        const float f0 = t16(e0, e2), f1 = t16(e1, e3);
        if (g < 3) CST8(sto, ((v2u){__float_as_uint(f0), __float_as_uint(f1)}));
        SB();
#pragma unroll
        for (int k = 32; k < 40; ++k) AC16(w1f[k], l16, w1o + (k & ~3) * 1024, (k & 3) * 1024);
        WAITV(8);
        if ((lane & 7) == 0) DMA4(L_DUMMY, xb0, a.ws);
        if ((lane & 7) == 0) DMA4(L_DUMMY, (lane & 8) && g < 3 ? sto : (xb1 & ~255u) + hi * 128, a.ws);
    }
    bar();
    if (t == 0) __hip_atomic_store((GAS u32*)(ws + gfl + 32768 + j * 128), tag, RLX_AGENT);
    TS(14);
    v4u wex;
    {
        ua_part(IC<UA1>{}, IC<32>{});
        WAITV(2 * TOUCH_OWN, W1T(32));
        ua_part(IC<32>{}, IC<40>{});
        {
            const u32 h0 = pk2(up[0], up[1]), h2 = pk2(up[2], 0.f);
            const u32 l0 = pk2(up[0] - bflo(h0), up[1] - bfhi(h0)), l2 = pk2(up[2] - bflo(h2), 0.f);
            wex.x = hi ? (b1v | (b1v << 16)) : ((h0 & 0xffffu) | (l0 << 16)); wex.y = hi ? 0u : ((h0 >> 16) | (l0 & 0xffff0000u));
            wex.z = hi ? 0u : ((h2 & 0xffffu) | (l2 << 16)); wex.w = hi ? 0u : (h2 & 0xffffu);
            TIE("+v"(wex));
        }
    }
    float cst[5];
    if (w == 7) {
#pragma unroll
        for (int i = 0; i < 5; ++i) cst[i] = 0;
#pragma unroll
        for (int r = 0; r < 2; ++r) { const int ch = lane + 64 * r; v4u gv = LD16(L_AUX + min(ch, 79) * 16), sv = LD16(L_AUX + 1280 + min(ch, 79) * 16);
#pragma unroll
            for (int e = 0; e < 4; ++e) { const u32 g_ = ch < 80 ? gv[e] : 0u, s_ = ch < 80 ? sv[e] : 0u;
                cst[0] = dot2(g_, ONE2, cst[0]); cst[1] = dot2(g_, g_, cst[1]); cst[2] = dot2(g_, s_, cst[2]); cst[3] = dot2(s_, ONE2, cst[3]); cst[4] = dot2(s_, s_, cst[4]); } }
#pragma unroll
        for (int i = 0; i < 5; ++i) { cst[i] = sum16(cst[i]); cst[i] = t16(cst[i], cst[i]); cst[i] = t32(cst[i], cst[i]); }
    }
    wait_flags(1, w, 1);
    TS(15);
    {
        v4u xi[13]; const u32 xb = WS_XG + grp * 81920 + 5 * w * 1024, sbo = WS_ST + (grp * 8 + w) * 4096;
#pragma unroll
        for (int i = 0; i < 3; ++i) AC16(xi[10 + i], l16, sbo, i * 1024);
#pragma unroll
        for (int i = 0; i < 10; ++i) AC16(xi[i], l16, xb + (i / 5) * 40960 + (i % 5 == 4 ? 4096 : 0), (i % 5 & 3) * 1024);
        WAITV(10, "+v"(xi[10]), "+v"(xi[11]), "+v"(xi[12]));
#pragma unroll
        for (int i = 0; i < 3; ++i) ST16(L_STP + w * 3072 + i * 1024 + l16, xi[10 + i]);
#pragma unroll
        for (int i = 0; i < 10; ++i) { WAITV(9 - i, "+v"(xi[i])); ST16(L_X + (i / 5) * 40960 + (5 * w + i % 5) * 1024 + l16, xi[i]); }
    }
    bar();
    TS(16);
    const int ks0 = w < 6 ? 7 * w : 42 + 6 * (w - 6), nks = w < 6 ? 7 : 6;
    v2u b2v[3];
    {
        const u32 w2o = WS_W2F + (5 * j * 54 + ks0) * 1024, w2l = WS_W2F + (5 * j * 54 + ks0 + nks - 1) * 1024;
        auto w2_load = [&](int k) { const int i = k / 5, nt = k % 5; if (i < 6) AC16R(w1f[k], l16, w2o + nt * 55296 + (i & ~3) * 1024, (i & 3) * 1024); else AC16R(w1f[k], l16, w2l + nt * 55296, 0); };
        f32x16 a0 = {}, a1 = {};
        if (w == 7) {
            __builtin_amdgcn_s_setprio(3);
            f32x2 us[3] = {};
#pragma unroll
            for (int i = 0; i < 48; ++i) us[i % 3] += *(const LAS f32x2*)(lds + L_STP + i * 512 + lane * 8);
            const f32x4 u0 = {us[0].x, us[0].y, us[1].x, us[1].y}; const f32x2 u1 = us[2];
            const float in = 1.f / 640.f, m1 = u0.x * in, m1b = bfr(m1), s1 = ln_s(fmaxf(u0.y * in - m1 * m1, 0.f)), r1 = 1.f / s1;
            const float m2 = (r1 * (u0.z - m1b * cst[0]) + cst[3]) * in;
            const float q2 = (r1 * r1 * (u0.w - 2.f * m1b * u1.x + m1b * m1b * cst[1]) + 2.f * r1 * (u1.y - m1b * cst[2]) + cst[4]) * in;
            const float s2 = ln_s(fmaxf(q2 - m2 * m2, 0.f)), bw = -bfr(m2) * s1, gm = s1 * s2;
            const u32 bh = pk2(bw, gm), bl = pk2(bw - bflo(bh), gm - bfhi(bh));
            const u32 xe = L_XE + hi * 1024 + l31 * 16;
            ST16(xe, ((v4u){pk2(-m1b, -m1b), pk2(s1, s1), (bh & 0xffffu) | (bh << 16), bl & 0xffffu}));
            ST16(xe + 512, ((v4u){(bh >> 16) | (bl & 0xffff0000u), 0u, 0u, 0u}));
            STF(L_A + lane * 4, 1.f / gm);
            asm volatile("" ::: "memory"); STF(L_MRED, 1.f);
            __builtin_amdgcn_s_setprio(0);
        }
        {
            u32 xa = L_X + l16, xb2 = L_X + 40960 + l16; TIE("+v"(xb2));
            v4u x0[3], x1[3];
#pragma unroll
            for (int ks = 0; ks < 2; ++ks) { x0[ks] = LD16(xa + ks * 1024); x1[ks] = LD16(xb2 + ks * 1024); }
#pragma unroll
            for (int ks = 0; ks < 40; ++ks) {
                if (ks + 2 < 40) { x0[(ks + 2) % 3] = LD16(xa + (ks + 2) * 1024); x1[(ks + 2) % 3] = LD16(xb2 + (ks + 2) * 1024); }
                SB();
                if (has_tile) { a0 = mfma32(w1f[ks], x0[ks % 3], a0); a1 = mfma32(w1f[ks], x1[ks % 3], a1); }
                SB();
                if (ks >= 1 && ks < 36) w2_load(ks - 1); }
        }
        { const u32 bo = (80 * j + par * 32 + g * 4) * 2; AG8(b2v[0], bo, a.b2, 0); AG8(b2v[1], bo, a.b2, 32); AG8(b2v[2], (80 * j + 64 + g * 4) * 2, a.b2, 0); }
        TS(17);
        u32 hb = WS_H + grp * 221184 + ((l31 >> 4) * 54 + nt0 + min(w, ntile - 1)) * 1024 + (l15 + 16 * hi) * 16;
        if (has_tile) {
            for (u32 sp = 0; sp < SPIN_CAP; ++sp) { if (*(volatile LAS float*)(lds + L_MRED) == 1.f) break; __builtin_amdgcn_s_sleep(1); }
            asm volatile("" ::: "memory");
            a0 = mfma32(wex, LD16(L_XE + l16), a0); a1 = mfma32(wex, LD16(L_XE + 1024 + l16), a1);
            const float A0 = LDF(L_A + l31 * 4), A1 = LDF(L_A + 128 + l31 * 4);
            const float k1 = -2.f * 0.7978845608028654f * 1.4426950408889634f, k3 = k1 * 0.044715f;
            auto gelu = [&](float z) { return z * __builtin_amdgcn_rcpf(1.f + __builtin_amdgcn_exp2f(z * (k1 + k3 * z * z))); };
#pragma unroll
            for (int tt = 0; tt < 2; ++tt)
#pragma unroll
                for (int hf = 0; hf < 2; ++hf) { v4u o; const float A = tt ? A1 : A0;
#pragma unroll
                    for (int e = 0; e < 4; ++e) { const int r = 8 * hf + 2 * e; o[e] = pk2(gelu(A * (tt ? a1[r] : a0[r])), gelu(A * (tt ? a1[r + 1] : a0[r + 1]))); }
                    CST16(hb + tt * 108 * 1024 + hf * 512, o); }
        }
        WAITV(0, W1T(0), W1T(8), "+v"(b2v[0]), "+v"(b2v[1]), "+v"(b2v[2])); TIE(W1T(16), W1T(24), "+v"(w1f[32]), "+v"(w1f[33]), "+v"(w1f[34]));
        if (has_tile && (lane & 7) < 4) DMA4(L_DUMMY, (hb & ~127u) + (lane & 1) * 512 + (lane & 2) * 54 * 1024, a.ws);
    }
    bar();
    if (t == 0) __hip_atomic_store((GAS u32*)(ws + gfl + 65536 + j * 128), tag, RLX_AGENT);
    TS(18);
    wait_flags(2, w, 1);
    TS(19);
    {
        f32x4 own[3]; v4u hf[14];
        const u32 hb = WS_H + grp * 221184 + ks0 * 1024;
        auto h_load = [&](int m, int i) { const u32 o = hb + ((mp + 1 + m) & 3) * 55296; v4u& d = hf[(m & 1) * 7 + i];
            if (i < 6) AC16(d, l16, o + (i & 4) * 1024, (i & 3) * 1024); else AC16(d, l16, o + (nks - 1) * 1024, 0); };
#pragma unroll
        for (int k = 0; k < 14; ++k) h_load(k / 7, k % 7);
#pragma unroll
        for (int m = 0; m < 4; ++m) { const int mt = (mp + 1 + m) & 3;
            f32x4 acc[5] = {};
#pragma unroll
            for (int i = 0; i < 7; ++i) { v4u& d = hf[(m & 1) * 7 + i];
                WAITV(m < 2 ? 13 : 27 - 7 * m - i, "+v"(d));
                if (i < 6 || nks == 7) {
#pragma unroll
                    for (int nt = 0; nt < 5; ++nt) acc[nt] = mfma16(w1f[i * 5 + nt], d, acc[nt]); }
                SB();
                if (m < 2) h_load(m + 2, i); }
            if (m == 3) {
                const u32 pb = L_PART + mt * 35840 + 6 * 5120 + l16;
                STF4(pb + (par ? 0 : 2048), par ? acc[0] : acc[2]); STF4(pb + (par ? 1024 : 3072), par ? acc[1] : acc[3]);
                if (hi != par) STF4(pb + 4096, acc[4]);
                own[0] = par ? acc[2] : acc[0]; own[1] = par ? acc[3] : acc[1]; own[2] = acc[4];
            } else {
                const u32 pb = L_PART + mt * 35840 + ((w - 2 * mt - 2) & 7) * 5120 + l16;
#pragma unroll
                for (int nt = 0; nt < 5; ++nt) STF4(pb + nt * 1024, acc[nt]);
            }
        }
        f32x4 gb[3], pr[3][7];
#pragma unroll
        for (int i = 0; i < 3; ++i) gb[i] = LDF4(L_OWN + 640 + ((i < 2 ? par * 32 + i * 16 : 64) + g * 4) * 4);
        bar();
        TS(20);
#pragma unroll
        for (int i = 0; i < 3; ++i)
#pragma unroll
            for (int k = 0; k < 7; ++k) pr[i][k] = LDF4(L_PART + mp * 35840 + (i < 2 ? 2 * par + i : 4) * 1024 + l16 + k * 5120);
        SB();
        GAS char* op = (GAS char*)a.out + (size_t)(row0 + 16 * mp + l15) * 1280 + j * 160 + g * 8;
#pragma unroll
        for (int i = 0; i < 3; ++i) {
            const f32x4 s = ((own[i] + pr[i][0]) + (pr[i][1] + pr[i][2])) + ((pr[i][3] + pr[i][4]) + (pr[i][5] + pr[i][6])); const v2u bb = b2v[i];
            const float o0 = bflo(x1p[2 * i]) + (s.x + bflo(bb.x)) * gb[i].x, o1 = bfhi(x1p[2 * i]) + (s.y + bfhi(bb.x)) * gb[i].y;
            const float o2 = bflo(x1p[2 * i + 1]) + (s.z + bflo(bb.y)) * gb[i].z, o3 = bfhi(x1p[2 * i + 1]) + (s.w + bfhi(bb.y)) * gb[i].w;
            if (i < 2 || hi == par) *(GAS v2u*)(op + (i < 2 ? par * 64 + i * 32 : 128)) = (v2u){pk2(o0, o1), pk2(o2, o3)};
        }
    }
    }
    TS(21); TC(31);
    asm volatile(".Lfb_code_b:");
}

extern "C" void kernel_launch(void* const* d_in, const int* in_sizes, int n_in, void* d_out, int out_size, void* d_ws, size_t ws_size, hipStream_t stream) {
    Args a;
    a.x = (const char*)d_in[0]; a.wq = (const char*)d_in[1]; a.wk = (const char*)d_in[2]; a.wv = (const char*)d_in[3]; a.wo = (const char*)d_in[4];
    a.w1 = (const char*)d_in[6]; a.b1 = (const char*)d_in[7]; a.w2 = (const char*)d_in[8]; a.b2 = (const char*)d_in[9]; a.c = (const char*)d_in[10]; a.wmod = (const char*)d_in[11];
    a.out = (char*)d_out; a.ws = (char*)d_ws;
    (void)hipFuncSetAttribute((const void*)fused_block, hipFuncAttributeMaxDynamicSharedMemorySize, L_TOTAL);
    hipLaunchKernelGGL(fused_block, dim3(256), dim3(512), L_TOTAL, stream, a);
}
```

```cpp
#include <hip/hip_runtime.h>
#include <hip/hip_bf16.h>
typedef unsigned u32; typedef unsigned long long u64; typedef unsigned short u16;
typedef u32 v4u __attribute__((ext_vector_type(4)));
typedef u32 v2u __attribute__((ext_vector_type(2)));
typedef float f32x2 __attribute__((ext_vector_type(2)));
typedef float f32x4 __attribute__((ext_vector_type(4)));
typedef float f32x16 __attribute__((ext_vector_type(16)));
typedef __bf16 bf16x8 __attribute__((ext_vector_type(8)));
typedef __bf16 bf16x2 __attribute__((ext_vector_type(2)));
typedef short s16x4 __attribute__((ext_vector_type(4)));
#define GAS __attribute__((address_space(1)))
#define LAS __attribute__((address_space(3)))
#define RLX_AGENT __ATOMIC_RELAXED, __HIP_MEMORY_SCOPE_AGENT
#define DI __device__ __forceinline__
#define SB() __builtin_amdgcn_sched_barrier(0)

constexpr u32 KVC = 12288;
constexpr u32 WS_E = 0, WS_MODG = 1024, WS_KVF = 16384, WS_WF = 18432, WS_GF = 32768, WS_KV = 163840, WS_O = WS_KV + 16 * KVC,
              WS_XG = WS_O + 32 * 81920, WS_ST = WS_XG + 32 * 81920, WS_H = WS_ST + 32 * 8 * 64 * 64, WS_W1F = WS_H + 32 * 221184,
              WS_W2F = WS_W1F + 2160 * 1024, WS_UA = WS_W2F + 2160 * 1024, WS_END = WS_UA + 54 * 1024, WS_DBG = 20u << 20;
constexpr u32 L_H = 0, L_WQ = 82944, L_WKV = 0, L_XK = 25600, L_PP = 139264, L_WO = 0, L_RING = 102400, L_WRED = 102400, L_X = 0, L_XE = 81920, L_STP = 126976, L_PART = 0,
              L_SA = 152064, L_G1 = 154624, L_AUX = 157184, L_MRED = 161024, L_A = 161536, L_OWN = 161792, L_DUMMY = 163328, L_TOTAL = 163840;
constexpr int HS = 1296;
constexpr int NPRE = 13;
#define QD 6
#define WD 6
#define KVF_I 6
#define POLLW 3
#define PSLEEP 4
#define TOUCH_OWN 1
#define UAK 16
#define WOD 8
#define MLD 0
#define KPRIO 1
constexpr int WPC = 7;
constexpr u32 SPIN_CAP = 400000, ONE2 = 0x3F803F80u;
template <int N> struct IC { static constexpr int value = N; };

DI float bflo(u32 v) { return __uint_as_float(v << 16); }
DI float bfhi(u32 v) { return __uint_as_float(v & 0xffff0000u); }
DI u32 pk2(float a, float b) { f32x2 v = {a, b}; return __builtin_bit_cast(u32, __builtin_convertvector(v, bf16x2)); }
DI float bfr(float a) { return bflo(pk2(a, a)); }
DI f32x16 mfma32(v4u a, v4u b, f32x16 c) { return __builtin_amdgcn_mfma_f32_32x32x16_bf16(__builtin_bit_cast(bf16x8, a), __builtin_bit_cast(bf16x8, b), c, 0, 0, 0); }
DI f32x4 mfma16(v4u a, v4u b, f32x4 c) { return __builtin_amdgcn_mfma_f32_16x16x32_bf16(__builtin_bit_cast(bf16x8, a), __builtin_bit_cast(bf16x8, b), c, 0, 0, 0); }
DI void bar() { asm volatile("s_waitcnt lgkmcnt(0)" ::: "memory"); __builtin_amdgcn_s_barrier(); asm volatile("" ::: "memory"); }
DI void vm0() { asm volatile("s_waitcnt vmcnt(0)" ::: "memory"); }
template <int CTRL> DI u32 dppu(u32 v) { return (u32)__builtin_amdgcn_update_dpp(0, (int)v, CTRL, 0xf, 0xf, true); }
template <int CTRL> DI float dpp(float v) { return __uint_as_float(dppu<CTRL>(__float_as_uint(v))); }
constexpr int X1 = 0xB1, X2 = 0x4E, HM = 0x141, R4 = 0x124, R8 = 0x128;
DI float t16(float a, float b) { auto r = __builtin_amdgcn_permlane16_swap(__float_as_uint(a), __float_as_uint(b), false, false); return __uint_as_float(r[0]) + __uint_as_float(r[1]); }
DI float t32(float a, float b) { auto r = __builtin_amdgcn_permlane32_swap(__float_as_uint(a), __float_as_uint(b), false, false); return __uint_as_float(r[0]) + __uint_as_float(r[1]); }
DI float hi32(float a) { auto r = __builtin_amdgcn_permlane32_swap(__float_as_uint(a), __float_as_uint(a), false, false); return __uint_as_float(r[1]); }
DI float sum16(float v) { v += dpp<X1>(v); v += dpp<X2>(v); v += dpp<R4>(v); v += dpp<R8>(v); return v; }
DI u32 umax16(u32 v) { v = max(v, dppu<X1>(v)); v = max(v, dppu<X2>(v)); v = max(v, dppu<R4>(v)); v = max(v, dppu<R8>(v)); return v; }
DI u32 umax64(u32 v) { v = umax16(v); auto r = __builtin_amdgcn_permlane16_swap(v, v, false, false); v = max(r[0], r[1]); auto q = __builtin_amdgcn_permlane32_swap(v, v, false, false); return max(q[0], q[1]); }

DI float dot2(u32 a, u32 b, float c) { return __builtin_amdgcn_fdot2_f32_bf16(__builtin_bit_cast(bf16x2, a), __builtin_bit_cast(bf16x2, b), c, false); }
DI u32 rfl(u32 v) { return (u32)__builtin_amdgcn_readfirstlane((int)v); }
DI float rdl(float v, int l) { return __uint_as_float((u32)__builtin_amdgcn_readlane((int)__float_as_uint(v), l)); }
DI float ln_s(float var) { return bfr(sqrtf(bfr(bfr(var) + 1e-5f))); }

struct Args { const char *x, *wq, *wk, *wv, *wo, *w1, *b1, *w2, *b2, *c, *wmod; char* out; char* ws; };

__global__ __launch_bounds__(512, 2) void fused_block(Args a) {
    extern __shared__ __attribute__((aligned(16))) char lds_[];
    LAS char* lds = (LAS char*)lds_;
    const u32 ldsb = (u32)(size_t)lds;
    const int t = threadIdx.x, lane = t & 63, w = __builtin_amdgcn_readfirstlane(t >> 6);
    const int b = blockIdx.x, xcd = b & 7, j = (b >> 3) & 7, grp = xcd * 4 + (b >> 6), row0 = grp * 64, mi = b >> 3;
    const int hi = lane >> 5, g = lane >> 4, l15 = lane & 15, l31 = lane & 31, q4 = (lane & 15) >> 2, p4 = lane & 3;
    GAS char* ws = (GAS char*)a.ws;
    const __amdgpu_buffer_rsrc_t rs = __builtin_amdgcn_make_buffer_rsrc(a.ws, 0, WS_END, 0x00020000);
#define LD16(off) (*(const LAS v4u*)(lds + (off)))
#define LD8(off) (*(const LAS v2u*)(lds + (off)))
#define LDF4(off) (*(const LAS f32x4*)(lds + (off)))
#define ST16(off, v) (*(LAS v4u*)(lds + (off)) = (v))
#define ST8(off, v) (*(LAS v2u*)(lds + (off)) = (v))
#define STF4(off, v) (*(LAS f32x4*)(lds + (off)) = (v))
#define STF(off, v) (*(LAS float*)(lds + (off)) = (v))
#define LDF(off) (*(const LAS float*)(lds + (off)))
#define TR(off) __builtin_bit_cast(v2u, __builtin_amdgcn_ds_read_tr16_b64_v4i16((LAS s16x4*)(lds + (off))))
#define GL8(p) (*(const GAS v2u*)(p))
#define CLD16(off) __builtin_amdgcn_raw_buffer_load_b128(rs, (off), 0, 16)
#define CST16(off, v) __builtin_amdgcn_raw_buffer_store_b128((v), rs, (off), 0, 16)
#define CST8(off, v) __builtin_amdgcn_raw_buffer_store_b64((v), rs, (off), 0, 16)
#define AG16(d, vo, sb, imm) asm volatile("global_load_dwordx4 %0, %1, %2 offset:%3" : "=v"(d) : "v"(vo), "s"(sb), "n"(imm))
#define AG8(d, vo, sb, imm) asm volatile("global_load_dwordx2 %0, %1, %2 offset:%3" : "=v"(d) : "v"(vo), "s"(sb), "n"(imm))
#define AG2(d, vo, sb, imm) asm volatile("global_load_ushort %0, %1, %2 offset:%3" : "=v"(d) : "v"(vo), "s"(sb), "n"(imm))
#define AC16(d, vo, so, imm) asm volatile("buffer_load_dwordx4 %0, %1, %2, %3 offen offset:%4 sc1" : "=v"(d) : "v"(vo), "s"(rs), "s"(so), "n"(imm))
#define AC16R(d, vo, so, imm) asm volatile("buffer_load_dwordx4 %0, %1, %2, %3 offen offset:%4 sc1" : "+v"(d) : "v"(vo), "s"(rs), "s"(so), "n"(imm))
#define AC8(d, vo, so, imm) asm volatile("buffer_load_dwordx2 %0, %1, %2, %3 offen offset:%4 sc1" : "=v"(d) : "v"(vo), "s"(rs), "s"(so), "n"(imm))
#define DMA16(dst, vo, sb) asm volatile("s_mov_b32 m0, %0\n\ts_nop 0\n\tglobal_load_lds_dwordx4 %1, %2" :: "s"(ldsb + (dst)), "v"(vo), "s"(sb) : "m0", "memory")
#define DMA4(dst, vo, sb) asm volatile("s_mov_b32 m0, %0\n\ts_nop 0\n\tglobal_load_lds_dword %1, %2" :: "s"(ldsb + (dst)), "v"(vo), "s"(sb) : "m0", "memory")
#define DMA16I(sl, il, vo, ig, sb) do { u32 tv_; asm volatile("s_add_u32 m0, %1, %2\n\tv_add_u32 %0, %4, %3\n\tglobal_load_lds_dwordx4 %0, %5" : "=&v"(tv_) : "s"(sl), "n"(il), "v"(vo), "n"(ig), "s"(sb) : "m0", "scc", "memory"); } while (0)
#define WAITV(n, ...) asm volatile("s_waitcnt vmcnt(%c[cnt])" : __VA_ARGS__ : [cnt] "n"(n) : "memory")
#define TIE(...) asm volatile("" : __VA_ARGS__)
#define TS(i)
#define TC(i)
    auto tr2 = [&](u32 o0, u32 o1) { v2u x0 = TR(o0), x1 = TR(o1); return (v4u){x0.x, x0.y, x1.x, x1.y}; };
    TS(0); TC(30);
    if (mi >= 28) {
        u64 pc; u32 len; asm volatile("s_getpc_b64 %0\n.Lfb_code_a:\n\ts_mov_b32 %1, .Lfb_code_b-.Lfb_code_a" : "=s"(pc), "=s"(len));
        const u32 vo = min((u32)((mi - 28) * 512 + t) * 64u, len & ~3u);
        DMA4(L_DUMMY, vo, pc);
    }
    u32 Ev; { const u32 z = 0; asm volatile("global_load_dword %0, %1, %2 sc1" : "=v"(Ev) : "v"(z), "s"(a.ws)); }
    const int m3 = mi / 3, mr3 = mi - 3 * m3; const bool crit = mr3 == 0 && mi < 30, defer = mr3 != 0 && mi < 30;
    const int mcol = mr3 == 0 ? xcd * 160 + min(m3, 9) * 16 : 1280 + xcd * 320 + (min(m3, 9) * 2 + mr3 - 1) * 16;
    auto mod_load = [&](int col, v4u (&wm)[3], u32 (&cc)[3], int part) {
        const u32 mr = t >> 1, r2 = min(mr + 512u, 639u), co = col * 2 + (t & 1) * 16;
        if (part <= 0) { AG2(cc[0], mr * 2, a.c, 0); AG2(cc[1], mr * 2, a.c, 512); AG2(cc[2], r2 * 2, a.c, 0); AG16(wm[0], mr * 7680 + co, a.wmod, 0); }
        if (part < 0 || part == 1) AG16(wm[1], (mr + 256) * 7680 + co, a.wmod, 0);
        if (part < 0 || part == 2) AG16(wm[2], r2 * 7680 + co, a.wmod, 0);
    };
    auto mod_partial = [&](v4u (&wm)[3], u32 (&cc)[3]) {
        float acc[8] = {};
#pragma unroll
        for (int i = 0; i < 3; ++i) {
            const float cv = bflo(cc[i]); u32 sl = pk2(cv * __builtin_amdgcn_rcpf(1.f + __builtin_amdgcn_exp2f(cv * -1.4426950408889634f)), 0.f);
            if (i == 2 && t >= 256) sl = 0u;
            const u32 sh = sl << 16;
#pragma unroll
            for (int e = 0; e < 4; ++e) { acc[2 * e] = dot2(wm[i][e], sl, acc[2 * e]); acc[2 * e + 1] = dot2(wm[i][e], sh, acc[2 * e + 1]); }
        }
#pragma unroll
        for (int i = 0; i < 4; ++i) acc[i] = t32(acc[i], acc[i + 4]);
#pragma unroll
        for (int i = 0; i < 2; ++i) { acc[i] = t16(acc[i], acc[i + 2]); acc[i] += dpp<X2>(acc[i]); acc[i] += dpp<R4>(acc[i]); acc[i] += dpp<R8>(acc[i]); }
        if ((lane & 14) == 0) *(LAS f32x2*)(lds + L_MRED + (w * 16 + 8 * (lane & 1) + 4 * hi + 2 * (g & 1)) * 4) = (f32x2){acc[0], acc[1]};
    };
    v4u wm[3], xr[10]; u32 cc[3];
    const int tq = j >> 1, isV = j & 1; float kn2 = 0;
    const u32 l16 = lane * 16;
    if (crit) { mod_load(mcol, wm, cc, -1); __builtin_amdgcn_s_barrier(); }
    { const u32 vo = (row0 + 8 * w) * 1280 + l16, vt = (row0 + 8 * w + 2 * g) * 1280 + 1024 + l15 * 16;
#pragma unroll
      for (int i = 0; i < 8; ++i) AG16(xr[i], vo + (i / 3) * 3840, a.x, (i % 3) * 1280);
      AG16(xr[8], vt, a.x, 0); AG16(xr[9], vt, a.x, 1280); }
    const u32 slice_vo = ((u32)lane / 10u) * 1120 + l16 + j * 160 + w * 7680, slice_sl = ldsb + w * 960;
#define SLICE(W, dst, k) do { SB(); if ((k) < 13) DMA16I(slice_sl, (dst) + (k) * 7680, slice_vo, (k) * 61440, W); \
                              else if (w < 2 || (w == 2 && lane < 40)) DMA16I(slice_sl, (dst) + (k) * 7680, slice_vo, (k) * 61440, W); SB(); } while (0)
    auto mod_publish = [&](int col, u32 tg) {
        if (lane < 8) { float s0 = 0, s1 = 0;
#pragma unroll
            for (int i = 0; i < 8; ++i) { const f32x2 v = *(const LAS f32x2*)(lds + L_MRED + (i * 16 + 2 * lane) * 4); s0 += v.x; s1 += v.y; }
            CST8(WS_MODG + (col / 2 + lane) * 8, ((v2u){pk2(s0, s1), tg})); } };
    TS(23);
    if (crit) { WAITV(10, "+v"(Ev), "+v"(wm[0]), "+v"(wm[1]), "+v"(wm[2]), "+v"(cc[0]), "+v"(cc[1]), "+v"(cc[2])); mod_partial(wm, cc); bar(); if (w == 0) mod_publish(mcol, rfl(Ev) * 1664525u + 1013904223u); }
    TS(1);
    SLICE(a.wq, L_WQ, 0); SLICE(a.wq, L_WQ, 1);
    WAITV(2);
    TIE("+v"(Ev), "+v"(xr[0]), "+v"(xr[1]), "+v"(xr[2]), "+v"(xr[3]), "+v"(xr[4]), "+v"(xr[5]), "+v"(xr[6]), "+v"(xr[7]), "+v"(xr[8]), "+v"(xr[9]));
    const u32 tag = rfl(Ev) * 1664525u + 1013904223u;
    v4u mg; const u32 mgo = min(t, 319) * 16;
    f32x2 xn[40];
    {
        float s[10], q[10];
        auto row = [&](int i) { s[i] = 0; q[i] = 0;
#pragma unroll
            for (int e = 0; e < 4; ++e) { s[i] = dot2(xr[i][e], ONE2, s[i]); q[i] = dot2(xr[i][e], xr[i][e], q[i]); } };
        row(0); row(1); SLICE(a.wq, L_WQ, 2); row(2); row(3); row(4); SLICE(a.wq, L_WQ, 3); row(5); row(6); row(7); SLICE(a.wq, L_WQ, 4); row(8); row(9);
#pragma unroll
        for (int i = 0; i < 4; ++i) { s[i] = t32(s[i], s[i + 4]); q[i] = t32(q[i], q[i + 4]); }
        SLICE(a.wq, L_WQ, 5);
        float rsd[2], nb[2];
#pragma unroll
        for (int i = 0; i < 2; ++i) { const float ss = sum16(t16(s[i], s[i + 2]) + s[8 + i]), qq = sum16(t16(q[i], q[i + 2]) + q[8 + i]);
            const float m = ss * (1.f / 640.f); rsd[i] = __builtin_amdgcn_rcpf(ln_s(fmaxf(qq * (1.f / 640.f) - m * m, 0.f))); nb[i] = -bfr(m) * rsd[i]; }
        auto nrm = [&](int i) { const float r_ = i < 8 ? rdl(rsd[i & 1], 16 * (i >> 1)) : rsd[i & 1], n_ = i < 8 ? rdl(nb[i & 1], 16 * (i >> 1)) : nb[i & 1];
#pragma unroll
            for (int e = 0; e < 4; ++e) xn[4 * i + e] = (f32x2){bflo(xr[i][e]), bfhi(xr[i][e])} * r_ + n_;
            TIE("+v"(xn[4 * i]), "+v"(xn[4 * i + 1]), "+v"(xn[4 * i + 2]), "+v"(xn[4 * i + 3])); };
        { const char* kp = isV ? a.wv : a.wk; const u32 sl = ldsb + w * 1024, vo = t * 16;
          DMA16I(sl, L_WKV, vo, 0, kp); nrm(0); DMA16I(sl, L_WKV + 8192, vo, 8192, kp); nrm(1); DMA16I(sl, L_WKV + 16384, vo, 16384, kp); if (w == 0) DMA16I(sl, L_WKV + 24576, vo, 24576, kp); }
        nrm(2); nrm(3); SLICE(a.wq, L_WQ, 6); nrm(4); nrm(5); nrm(6); SLICE(a.wq, L_WQ, 7); nrm(7); nrm(8); nrm(9); if (lane < 60) SLICE(a.wq, L_WQ, 8);
    }
    if ((w >> 1) == tq) {
#pragma unroll
        for (int i = 0; i < 10; ++i) { v4u o;
#pragma unroll
            for (int e = 0; e < 4; ++e) o[e] = pk2(xn[4 * i + e].x, xn[4 * i + e].y);
            ST16(i < 8 ? L_XK + (8 * (w & 1) + i) * HS + l16 : L_XK + (8 * (w & 1) + 2 * g + i - 8) * HS + 1024 + l15 * 16, o); } }
    TS(2);
    WAITV(3); bar();
    AC16(mg, mgo, WS_MODG, 0);
    TS(3);
    const int hh = w & 3, th = w >> 2, ks2 = min(w + 16, 19);
    v4u wf[40], kf[3][2]; f32x2 xk[12];
    { const u32 wb = L_WKV + (8 * g + q4) * 40 + p4 * 8;
#pragma unroll
      for (int i = 0; i < 3; ++i) { const u32 o = wb + (i < 2 ? w + 8 * i : ks2) * 1280;
          const v4u xv = LD16(L_XK + l15 * HS + g * 16 + (i < 2 ? w + 8 * i : ks2) * 64);
          kf[i][0] = tr2(o, o + 160); kf[i][1] = tr2(o + 32, o + 192);
#pragma unroll
          for (int e = 0; e < 4; ++e) xk[4 * i + e] = (f32x2){bflo(xv[e]), bfhi(xv[e])};
          TIE("+v"(xk[4 * i]), "+v"(xk[4 * i + 1]), "+v"(xk[4 * i + 2]), "+v"(xk[4 * i + 3])); } }
    const u32 wqb = L_WQ + (8 * hi + q4) * 160 + (20 * hh + 16 * (g & 1) + 4 * p4) * 2; u32 wq2 = wqb + 20 * 2560; TIE("+v"(wq2));
    auto wq_frag = [&](int ks) { const int k = ks < 27 ? ks : ks - 27; const u32 o = k < 20 ? wqb + k * 2560 : wq2 + (k - 20) * 2560; wf[ks] = tr2(o, o + 640); };
#pragma unroll
    for (int ks = 0; ks < NPRE; ++ks) wq_frag(ks);
    WAITV(0, "+v"(mg));
    TS(26);
    if (w < 5) {
        for (u32 sp = 0; sp < SPIN_CAP; ++sp) { if (__all(mg.y == tag && mg.w == tag)) break; AC16(mg, mgo, WS_MODG, 0); WAITV(0, "+v"(mg)); }
        f32x4 f = {bflo(mg.x), bfhi(mg.x), bflo(mg.z), bfhi(mg.z)};
        if (t >= 160) { const u32 p0 = pk2(1.f + f.x, 1.f + f.y), p1 = pk2(1.f + f.z, 1.f + f.w); f = (f32x4){bflo(p0), bfhi(p0), bflo(p1), bfhi(p1)}; }
        STF4(L_SA + t * 16, f);
    }
    bar();
    TS(4);
    {
        f32x4 acc0 = {}, acc1 = {}, gv[6], sv[6];
#pragma unroll
        for (int i = 0; i < 6; ++i) { const u32 mo = (i < 4 ? w + 8 * (i >> 1) : ks2) * 128 + g * 32 + (i & 1) * 16; gv[i] = LDF4(L_G1 + mo); sv[i] = LDF4(L_SA + mo); }
        SB();
#pragma unroll
        for (int i = 0; i < 3; ++i) { v4u hf;
#pragma unroll
            for (int e = 0; e < 4; ++e) { const f32x4 gg = gv[2 * i + (e >> 1)], ss = sv[2 * i + (e >> 1)]; const f32x2 v = xk[4 * i + e] * (e & 1 ? gg.zw : gg.xy) + (e & 1 ? ss.zw : ss.xy); hf[e] = pk2(v.x, v.y); }
            if (i == 2 && w >= 4) hf = (v4u){};
            if (isV) { acc0 = mfma16(hf, kf[i][0], acc0); acc1 = mfma16(hf, kf[i][1], acc1); } else { acc0 = mfma16(kf[i][0], hf, acc0); acc1 = mfma16(kf[i][1], hf, acc1); } }
        STF4(L_WKV + w * 1280 + l16, acc0);
        if (isV ? l15 < 4 : g == 0) STF4(L_WKV + w * 1280 + 1024 + (isV ? 4 * g + l15 : l15) * 16, acc1);
    }
    const int key0 = row0 + 16 * tq; const u32 cb = WS_KV + (key0 >> 7) * KVC, tile = (key0 >> 5) & 3, kk = (key0 & 31) + l15, vb = cb + 4096 + tile * 1536 + (tq & 1) * 768;
    u32 kvs0 = isV ? vb + (g & 1) * 384 + l15 * 16 + 8 * (g >> 1) : cb + tile * 1024 + (kk + 32 * (g & 1)) * 16 + 8 * (g >> 1);
    u32 kvs1 = isV ? vb + (q4 & 1) * 384 + (16 + 4 * g + p4) * 16 + 8 * (q4 >> 1) : cb + 10240 + tile * 512 + kk * 16, kvr = L_WKV + g * 1280 + 1024 + l15 * 16;
    TIE("+v"(kvs0), "+v"(kvs1), "+v"(kvr));
    bar();
    TS(5);
    if (w == 0) {
        __builtin_amdgcn_s_setprio(3);
        f32x4 r0[6];
#pragma unroll
        for (int k = 0; k < 6; ++k) r0[k] = LDF4(L_WKV + k * 1280 + l16);
        SB();
        r0[0] += r0[1]; r0[2] += r0[3]; r0[1] = LDF4(L_WKV + 6 * 1280 + l16); r0[3] = LDF4(L_WKV + 7 * 1280 + l16); SB();
        r0[0] += r0[4]; r0[2] += r0[5];
        f32x4 s1 = LDF4(kvr), s2 = LDF4(kvr + 5120); SB();
        const f32x4 s0 = (r0[0] + r0[1]) + (r0[2] + r0[3]);
        const v2u p0 = {pk2(s0.x, s0.y), pk2(s0.z, s0.w)};
        CST8(kvs0, p0); SB();
        s1 += s2;
        const float u0 = t32(s1.x, s1.y), u1 = t32(s1.z, s1.w), u2 = t16(u0, u1);
        const auto uy = __builtin_amdgcn_permlane32_swap(__float_as_uint(u2), __float_as_uint(u2), false, false);
        const u32 pa = pk2(__uint_as_float(uy[0]), __uint_as_float(uy[1]));
        const auto pz = __builtin_amdgcn_permlane16_swap(pa, pa, false, false);
        v2u p1 = {pz[0], pz[1]};
        if (isV) {
            if (g) { const u32 c = p4 == 0 ? ONE2 : 0u; p1 = (v2u){c, c}; }
            if (g < 2) CST8(kvs1, p1);
        } else {
            if (g == 0) CST16(kvs1, ((v4u){p1.x, p1.y, ONE2, ONE2}));
            kn2 = dot2(p0.x, p0.x, dot2(p0.y, p0.y, 0.f));
            kn2 = t16(kn2, kn2); kn2 = t32(kn2, kn2);
            kn2 = dot2(p1.x, p1.x, dot2(p1.y, p1.y, kn2));
            kn2 = __uint_as_float(umax16(__float_as_uint(kn2)));
        }
    }
    TS(6);
    {
        const u32 hb0 = L_H + 8 * w * HS + l16, ht0 = L_H + (8 * w + 2 * g) * HS + 1024 + l15 * 16;
        f32x4 gm[2], sm[2], gt[2], st[2];
#pragma unroll
        for (int k = 0; k < 2; ++k) { gm[k] = LDF4(L_G1 + lane * 32 + k * 16); sm[k] = LDF4(L_SA + lane * 32 + k * 16); }
        SLICE(a.wq, L_WQ - 69120, 9); SLICE(a.wq, L_WQ - 69120, 10);
#pragma unroll
        for (int i = 0; i < 10; ++i) { v4u o;
            if (i == 4) {
#pragma unroll
                for (int k = 0; k < 2; ++k) { gt[k] = LDF4(L_G1 + 2048 + l15 * 32 + k * 16); st[k] = LDF4(L_SA + 2048 + l15 * 32 + k * 16); }
                SLICE(a.wq, L_WQ - 69120, 12); SLICE(a.wq, L_WQ - 69120, 13); }
            if (i == 2) SLICE(a.wq, L_WQ - 69120, 11);
#pragma unroll
            for (int e = 0; e < 4; ++e) { const f32x4 gg = i < 8 ? gm[e >> 1] : gt[e >> 1], ss = i < 8 ? sm[e >> 1] : st[e >> 1];
                const f32x2 v = xn[4 * i + e] * (e & 1 ? gg.zw : gg.xy) + (e & 1 ? ss.zw : ss.xy); o[e] = pk2(v.x, v.y); }
            if (i == KVF_I && w == 0) { WAITV(5); if (lane == 0) CST8(WS_KVF + b * 8, ((v2u){__float_as_uint(kn2), tag})); __builtin_amdgcn_s_setprio(0); TS(7); }
            ST16(i >= 8 ? ht0 + (i - 8) * HS : hb0 + i * HS, o); SB(); }
    }
    if (w == 0) WAITV(1); else WAITV(0);
    bar();
    TS(24);
    v4u wm2[3]; u32 cc2[3];
    u32 ppd[3], ppv[3]; v4u ppf[3];
    const bool pp3 = w == 1 && b < 224, pp3w2 = b >= 112;
    auto pp_addr = [&](int r) { const int i = r < 2 ? b * 8 + w : 2048 + min(pp3w2 ? b - 112 : b, 111); const bool isw2 = r < 2 ? r == 1 : pp3w2;
        if (isw2) { const int ks = i / 40, nt = i - ks * 40; ppv[r] = ks * 40960 + nt * 32 + (lane >> 1) * 1280 + (lane & 1) * 16; ppd[r] = WS_W2F + (nt * 54 + ks) * 1024; }
        else { const int ks = i / 54, nt = i - ks * 54, jj = ks / 5, ii = ks - jj * 5, lr = lane >> 2;
            ppv[r] = (80 * jj + (ii < 4 ? 32 * (ii >> 1) + 8 * (ii & 1) : 64)) * 3456 + nt * 64 + (lr + (ii < 4 ? (lr & 8) : 0)) * 3456 + p4 * 16; ppd[r] = WS_W1F + (nt * 40 + ks) * 1024; }
        ppd[r] = rfl(ppd[r]); TIE("+v"(ppv[r]), "+s"(ppd[r])); };
    auto pp_load = [&](int r) { DMA16(L_PP + (r < 2 ? w + 8 * r : 16) * 1024, ppv[r], (r < 2 ? r == 1 : pp3w2) ? a.w2 : a.w1); };
    auto pp_read = [&](int r) { const bool isw2 = r < 2 ? r == 1 : pp3w2;
        const u32 o = L_PP + (r < 2 ? w + 8 * r : 16) * 1024 + (isw2 ? (16 * (g >> 1) + 4 * (g & 1) + q4) * 32 + p4 * 8 : (4 * hi + q4) * 64 + (g & 1) * 32 + p4 * 8);
        ppf[r] = tr2(o, o + (isw2 ? 256 : 512)); };
    auto pp_store = [&](int r) { __builtin_amdgcn_raw_buffer_store_b128(ppf[r], rs, l16, ppd[r], 16); };
    v4u qf0, qf1;
#define TOUCH(vo, so) asm volatile("s_mov_b32 m0, %0\n\ts_nop 0\n\tbuffer_load_dword %1, %2, %3 offen lds" :: "s"(ldsb + L_DUMMY), "v"((u32)(vo)), "s"(rs), "s"(so) : "m0", "memory")
    const u32 kvo0 = t * 16;
#define KVDMA(sl, il, sg, ig) do { u32 ts_; asm volatile("s_add_u32 m0, %1, %2\n\ts_add_u32 %0, %3, %4\n\tbuffer_load_dwordx4 %5, %6, %0 offen sc1 lds" : "=&s"(ts_) : "s"(sl), "n"(il), "s"(sg), "n"(ig), "v"(l16), "s"(rs) : "m0", "scc", "memory"); } while (0)
    {
        f32x16 acc = {}; v4u hq[QD];
        const u32 hb = L_H + (32 * th + l31) * HS + hi * 16;
#pragma unroll
        for (int ks = 0; ks < QD; ++ks) hq[ks] = LD16(hb + ks * 32);
#pragma unroll
        for (int ks = 0; ks < 40; ++ks) {
            SB(); acc = mfma32(wf[ks], hq[ks % QD], acc); SB();
            if (ks + QD < 40) hq[ks % QD] = LD16(hb + (ks + QD) * 32);
            if (ks + WD >= NPRE && ks + WD < 40) wq_frag(ks + WD);
            if (ks == 1 || ks == 3 || ks == 5) pp_addr(ks >> 1);
            if (ks == MLD && defer) mod_load(mcol, wm2, cc2, -1);
        }
        SB();
        const float sc = 0.22360679774997896f * 1.4426950408889634f;
        qf0 = (v4u){pk2(acc[0] * sc, acc[1] * sc), pk2(acc[2] * sc, acc[3] * sc), pk2(acc[4] * sc, acc[5] * sc), pk2(acc[6] * sc, acc[7] * sc)};
        qf1 = (v4u){hi ? 0u : pk2(acc[8] * sc, acc[9] * sc), hi ? 0u : pk2(acc[10] * sc, acc[11] * sc), 0u, 0u};
        float qn2 = dot2(qf0.x, qf0.x, dot2(qf0.y, qf0.y, dot2(qf0.z, qf0.z, dot2(qf0.w, qf0.w, dot2(qf1.x, qf1.x, dot2(qf1.y, qf1.y, 0.f))))));
        qn2 = t32(qn2, qn2);
        TS(8);
        if (defer) { WAITV(0, "+v"(wm2[0]), "+v"(wm2[1]), "+v"(wm2[2]), "+v"(cc2[0]), "+v"(cc2[1]), "+v"(cc2[2])); mod_partial(wm2, cc2); }
        v4u kv0[6];
        if (w < 4) {
            v4u f0, f1;
            for (u32 sp = 0; sp < SPIN_CAP; ++sp) { f0 = CLD16(WS_KVF + l16); f1 = CLD16(WS_KVF + 1024 + l16);
                if (__all(f0.y == tag && f0.w == tag && f1.y == tag && f1.w == tag)) break;
                __builtin_amdgcn_s_sleep(PSLEEP); }
#pragma unroll
            for (int i = 0; i < 6; ++i) AC16(kv0[i], kvo0, WS_KV + i * 4096, 0);
            const u32 km = umax64(max(max(f0.x, f0.z), max(f1.x, f1.z)));
            if (t == 0) STF(L_A, __uint_as_float(km));
        }
        TS(9);
        bar();
        if (w < 4) {
            WAITV(3, "+v"(kv0[0]), "+v"(kv0[1]), "+v"(kv0[2]));
#pragma unroll
            for (int i = 0; i < 3; ++i) ST16(L_RING + i * 4096 + kvo0, kv0[i]);
            WAITV(0, "+v"(kv0[3]), "+v"(kv0[4]), "+v"(kv0[5]));
#pragma unroll
            for (int i = 3; i < 6; ++i) ST16(L_RING + i * 4096 + kvo0, kv0[i]);
        }
        const float kmax2 = LDF(L_A);
        const float Msh = bfr(__builtin_amdgcn_sqrtf(qn2 * kmax2) * 1.01f + 1e-30f);
        qf1.z = hi ? 0u : (pk2(-Msh, 0.f) & 0xffffu);
        TS(25);
    }
    bar();
    if (b == 0 && t == 128) __hip_atomic_store((GAS u32*)(ws + WS_E), tag, RLX_AGENT);
    TS(10); TC(28);
    const int ntile = j < 6 ? 7 : 6, nt0 = j < 6 ? 7 * j : 42 + 6 * (j - 6); const bool has_tile = w < ntile;
    const u32 w1o = WS_W1F + (nt0 + min(w, ntile - 1)) * 40960;
    v4u w1f[40];
    f32x16 oacc = {};
    {
        const u32 ak0 = L_RING + lane * 16, ak1 = L_RING + 10240 + l31 * 16, av = L_RING + 4096 + (hi * 24 + (l31 < 24 ? l31 : (l31 < 28 ? l31 - 4 : l31 - 12))) * 16;
        v4u k0 = LD16(ak0), k1 = LD16(ak1);
        f32x16 sc = mfma32(k0, qf0, (f32x16){}); sc = mfma32(k1, qf1, sc);
        k0 = LD16(ak0 + 1024); k1 = LD16(ak1 + 512);
        v4u p1 = {}, v1 = {}, pf0, pf1;
        const u32 kvl = ldsb + L_RING + w * 1024, kvg = WS_KV + w * 1024, tw2 = WS_W2F + mi * 71680 + w * 1280;
#define EX(i) __builtin_amdgcn_exp2f(sc[i])
        const int mu = t - 320; const u32 mgo2 = (t < 320 ? 1920 + 4 * t : (mu < 20 ? 1280 + 80 * j + 4 * mu : 3200 + 80 * j + 4 * (min(mu, 39) - 20))) * 4;
        auto chunk = [&](auto CC) {
            constexpr int c = decltype(CC)::value; constexpr u32 cur = (c % 3) * KVC, nxt = ((c + 1) % 3) * KVC, fre = ((c + 2) % 3) * KVC;
            constexpr int NY = c == 2 ? 4 : (c < 7 ? 2 : (c == 7 ? 1 : 5));
#pragma unroll
            for (int i = 0; i < 4; ++i) {
                v4u p0; f32x16 sn; float e[16];
                const u32 kb = i < 2 ? cur + (i + 2) * 1024 : nxt + (i - 2) * 1024, kb1 = i < 2 ? cur + (i + 2) * 512 : nxt + (i - 2) * 512;
                sn = mfma32(k0, qf0, (f32x16){}); SB();
                const v4u v0 = LD16(cur + av + i * 1536); k0 = LD16(kb + ak0); SB();
                e[0] = EX(0); e[1] = EX(1); e[2] = EX(2); e[3] = EX(3); SB();
                oacc = mfma32(v1, p1, oacc); SB();
                v1 = LD16(cur + av + i * 1536 + 768); SB();
                e[4] = EX(4); e[5] = EX(5); SB(); p0.x = pk2(e[0], e[1]); p0.y = pk2(e[2], e[3]); SB(); e[6] = EX(6); SB();
                sn = mfma32(k1, qf1, sn); SB();
                k1 = LD16(kb1 + ak1); SB();
                e[7] = EX(7); e[8] = EX(8); SB(); p0.z = pk2(e[4], e[5]); SB(); e[9] = EX(9); SB(); p0.w = pk2(e[6], e[7]); SB(); e[10] = EX(10); e[11] = EX(11); SB();
                oacc = mfma32(v0, p0, oacc); SB();
                e[12] = EX(12); e[13] = EX(13); SB(); p1.x = pk2(e[8], e[9]); SB(); e[14] = EX(14); e[15] = EX(15); SB(); p1.y = pk2(e[10], e[11]); p1.z = pk2(e[12], e[13]); p1.w = pk2(e[14], e[15]);
                TIE("+v"(p1));
                if (i == 0) {
                    if constexpr (c == 4) { if (t == 64) CST8(WS_WF + b * 8, ((v2u){0u, tag})); }
                    if constexpr (c == 3) { if (defer && w == 7) mod_publish(mcol, tag); }
                    if constexpr (c == WPC) { if (w == 7) { AC16(pf0, l16, WS_WF, 0); AC16(pf1, l16, WS_WF, 1024); } }
                    if constexpr (c < 14) { if (w < 4) KVDMA(kvl, fre + 8192, kvg, (c + 2) * KVC + 8192); KVDMA(kvl, fre, kvg, (c + 2) * KVC); }
                    if constexpr (c == 2) { pp_read(0); pp_read(1); pp_read(2); }
                }
                if (i == 1) {
                    if constexpr (c == 2) { pp_store(0); pp_store(1); if (pp3) pp_store(2); }
                    if constexpr (c == 14) AC16(mg, mgo2, WS_MODG, 0);
                }
                if (i == 3) { if constexpr (c == WPC) { if (w == 7) {
                    WAITV(2, "+v"(pf0), "+v"(pf1));
                    for (u32 sp = 0; sp < SPIN_CAP; ++sp) { if (__all(pf0.y == tag && pf0.w == tag && pf1.y == tag && pf1.w == tag)) break;
                        __builtin_amdgcn_s_sleep(4); AC16(pf0, l16, WS_WF, 0); AC16(pf1, l16, WS_WF, 1024); WAITV(0, "+v"(pf0), "+v"(pf1)); } } } }
                if (i >= 2) {
                    if constexpr (c == 0) { pp_load(i - 2); if (i == 3 && pp3) pp_load(2); }
                    if constexpr (c >= 1 && c < 8) { if (i == 2) SLICE(a.wo, L_WO, 2 * c - 2); else SLICE(a.wo, L_WO, 2 * c - 1); }
                    if constexpr (c > WPC) { const int k = 4 * (c - WPC - 1) + 2 * (i - 2); AC16(w1f[k], l16, w1o + (k & ~3) * 1024, (k & 3) * 1024); AC16(w1f[k + 1], l16, w1o + (k & ~3) * 1024, ((k + 1) & 3) * 1024); }
                    if constexpr (c > WPC && c < WPC + 8) { if (i == 3 && lane < 10) TOUCH(l16 * 8, tw2 + (c - WPC - 1) * 10240); }
                }
                SB();
                sc = sn;
            }
            if constexpr (c < 14) WAITV(NY);
            bar();
        };
        chunk(IC<0>{}); chunk(IC<1>{}); chunk(IC<2>{}); chunk(IC<3>{}); chunk(IC<4>{}); chunk(IC<5>{}); chunk(IC<6>{}); chunk(IC<7>{});
        chunk(IC<8>{}); chunk(IC<9>{}); chunk(IC<10>{}); chunk(IC<11>{}); chunk(IC<12>{}); chunk(IC<13>{}); chunk(IC<14>{}); chunk(IC<15>{});
        oacc = mfma32(v1, p1, oacc);
    }
    TS(11); TC(29);
    {
    int t2 = threadIdx.x; TIE("+v"(t2));
    const int t = t2, lane = t & 63, hi = lane >> 5, g = lane >> 4, l15 = lane & 15, l31 = lane & 31, q4 = (lane & 15) >> 2, p4 = lane & 3; const u32 l16 = lane * 16;
    float lsum = hi32(oacc[8]);
    if (__any(!(lsum >= 1e-12f && lsum < 3e38f))) {
        qf1.z = 0u;
        const u32 ak0 = WS_KV + lane * 16, ak1 = WS_KV + 10240 + l31 * 16, av = WS_KV + 4096 + (hi * 24 + (l31 < 24 ? l31 : 20)) * 16;
        float mx = -__builtin_inff();
        for (int tI = 0; tI < 64; ++tI) { const u32 cb = (tI >> 2) * KVC, i = tI & 3;
            const v4u k0 = CLD16(cb + ak0 + i * 1024), k1 = CLD16(cb + ak1 + i * 512);
            f32x16 s = mfma32(k0, qf0, (f32x16){}); s = mfma32(k1, qf1, s);
#pragma unroll
            for (int r = 0; r < 16; ++r) mx = fmaxf(mx, s[r]); }
        mx = fmaxf(mx, __shfl_xor(mx, 32));
        oacc = (f32x16){};
        for (int tI = 0; tI < 64; ++tI) { const u32 cb = (tI >> 2) * KVC, i = tI & 3;
            const v4u k0 = CLD16(cb + ak0 + i * 1024), k1 = CLD16(cb + ak1 + i * 512);
            const v4u v0 = CLD16(cb + av + i * 1536), v1 = CLD16(cb + av + i * 1536 + 768);
            f32x16 s = mfma32(k0, qf0, (f32x16){}); s = mfma32(k1, qf1, s);
            v4u p0, p1;
#pragma unroll
            for (int e = 0; e < 4; ++e) { p0[e] = pk2(__builtin_amdgcn_exp2f(s[2 * e] - mx), __builtin_amdgcn_exp2f(s[2 * e + 1] - mx));
                                          p1[e] = pk2(__builtin_amdgcn_exp2f(s[8 + 2 * e] - mx), __builtin_amdgcn_exp2f(s[9 + 2 * e] - mx)); }
            oacc = mfma32(v0, p0, oacc); oacc = mfma32(v1, p1, oacc); }
        lsum = hi32(oacc[8]);
    }
    const u32 gfl = WS_GF + grp * 1024;
    const int mp = w >> 1, par = w & 1;
    v2u xres[3]; u32 b1v;
    {
        const float il = __builtin_amdgcn_rcpf(lsum); const int mt = 2 * th + (l31 >> 4);
        const v4u o0 = {pk2(oacc[0] * il, oacc[1] * il), pk2(oacc[2] * il, oacc[3] * il), pk2(oacc[4] * il, oacc[5] * il), pk2(oacc[6] * il, oacc[7] * il)};
        const u32 ob = WS_O + grp * 81920 + mt * 20480 + l15 * 16;
        CST16(ob + (2 * j + (hh >> 1)) * 1024 + (2 * (hh & 1) + hi) * 256, o0);
        const int ci = 64 + 2 * j + (hh >> 1);
        if (!hi) CST8(ob + ci * 256 + 8 * (hh & 1), ((v2u){pk2(oacc[8] * il, oacc[9] * il), pk2(oacc[10] * il, oacc[11] * il)}));
        SB();
        const u32 xo = (row0 + 16 * mp + l15) * 1280 + j * 160 + g * 8;
        AG8(xres[0], xo + par * 64, a.x, 0); AG8(xres[1], xo + par * 64, a.x, 32); AG8(xres[2], xo, a.x, 128);
        AG2(b1v, ((nt0 + min(w, ntile - 1)) * 32 + l31) * 2, a.b1, 0);
    }
    {
        WAITV(6, "+v"(mg));
        if (t == 511) STF(L_MRED, 0.f);
        const int mu = t - 320; const u32 mgo2 = (t < 320 ? 1920 + 4 * t : (mu < 20 ? 1280 + 80 * j + 4 * mu : 3200 + 80 * j + 4 * (min(mu, 39) - 20))) * 4;
                        if (w < 6) {
                            for (u32 sp = 0; sp < SPIN_CAP; ++sp) { if (__all(mg.y == tag && mg.w == tag)) break; AC16(mg, mgo2, WS_MODG, 0); WAITV(0, "+v"(mg)); }
                            v2u v = {mg.x, mg.z}; f32x4 f = {bflo(v.x), bfhi(v.x), bflo(v.y), bfhi(v.y)};
                            if (w < 5) {
                                const int isg = t >= 160, tp = t - 160 * isg, jj = (tp * 205) >> 12, r4 = tp - jj * 20;
                                if (isg) { v = (v2u){pk2(1.f + f.x, 1.f + f.y), pk2(1.f + f.z, 1.f + f.w)}; f = (f32x4){bflo(v.x), bfhi(v.x), bflo(v.y), bfhi(v.y)}; }
                                const int o = r4 < 16 ? (r4 >> 3) * 64 + (r4 & 3) * 16 + (r4 & 4) * 2 : 128 + (r4 & 1) * 16 + (r4 & 2) * 4;
                                ST8(L_AUX + (isg ? 0 : 1280) + jj * 160 + o, v);
                                if (jj == j) { if (isg) STF4(L_OWN + 320 + r4 * 16, f); ST8(L_OWN + (isg ? 960 : 1120) + r4 * 8, v); }
                            } else if (mu < 40) STF4(L_OWN + (mu < 20 ? mu * 16 : 640 + (mu - 20) * 16), f);
                        } else if (t < 464) ST16(L_AUX + 2560 + (t - 384) * 16, ((v4u){ONE2, ONE2, ONE2, ONE2}));
    }
#define W1T(k) "+v"(w1f[k]), "+v"(w1f[k + 1]), "+v"(w1f[k + 2]), "+v"(w1f[k + 3]), "+v"(w1f[k + 4]), "+v"(w1f[k + 5]), "+v"(w1f[k + 6]), "+v"(w1f[k + 7])
    WAITV(4, W1T(0), W1T(8)); TIE(W1T(16), W1T(24));
    if (lane < 12) DMA4(L_DUMMY, WS_O + grp * 81920 + (2 * th + (lane & 1)) * 20480 + (lane < 8 ? (2 * j + (hh >> 1)) * 1024 + (hh & 1) * 512 + (lane >> 1) * 128 : (64 + 2 * j + (hh >> 1)) * 256 + (lane & 2) * 64), a.ws);
    bar();
    if (t == 0) __hip_atomic_store((GAS u32*)(ws + gfl + j * 128), tag, RLX_AGENT);
    TS(12);
    auto wait_flags = [&](int phase, int first, int n) {
        for (u32 sp = 0; sp < SPIN_CAP; ++sp) { const u32 f = __builtin_amdgcn_raw_buffer_load_b32(rs, gfl + phase * 32768 + (first + (lane & (n - 1))) * 128, 0, 16);
            if (__all(f == tag)) break; __builtin_amdgcn_s_sleep(1); } };
    const bool ua_own = has_tile && ((nt0 + w) & 31) == grp; const u32 uao = WS_UA + (nt0 + min(w, ntile - 1)) * 1024, uav = l31 * 32;
    const u32 ab = L_AUX + min(l31, 2) * 1280 + hi * 16;
    float up[3] = {};
    auto ua_part = [&](auto K0, auto K1) { constexpr int k0 = decltype(K0)::value, k1 = decltype(K1)::value; f32x16 ua = {}; v4u af[4];
#pragma unroll
        for (int ks = k0; ks < k0 + 4; ++ks) af[ks & 3] = LD16(ab + ks * 32);
#pragma unroll
        for (int ks = k0; ks < k1; ++ks) { SB(); ua = mfma32(af[ks & 3], w1f[ks], ua); SB(); if (ks + 4 < k1) af[ks & 3] = LD16(ab + (ks + 4) * 32); }
#pragma unroll
        for (int i = 0; i < 3; ++i) up[i] = k0 ? up[i] + ua[i] : ua[i];
        TIE("+v"(up[0]), "+v"(up[1]), "+v"(up[2])); };
    if (ua_own) ua_part(IC<0>{}, IC<32>{});
    u32 x1p[6];
    {
        v4u xf[10], wr[WOD]; f32x4 acc[5] = {};
        const u32 wm_ = L_WO + (320 * par + 20 * (g >> 1) + 4 * (g & 1) + q4) * 160 + p4 * 8, wl_ = L_WO + (320 * par + 40 * g + 16 + q4) * 160 + p4 * 8;
        auto wo_frag = [&](int s_) { const int i = s_ / 5, nt = s_ % 5; const u32 o = i < 8 ? wm_ + i * 6400 + nt * 32 : wl_ + (i - 8) * 25600 + nt * 32; wr[s_ % WOD] = tr2(o, o + (i < 8 ? 1280 : 3200)); };
#pragma unroll
        for (int s_ = 0; s_ < WOD; ++s_) wo_frag(s_);
        SB();
        wait_flags(0, 4 * par, 4);
        TS(13);
        { const u32 ob = WS_O + grp * 81920 + mp * 20480 + par * 8192, ot = WS_O + grp * 81920 + mp * 20480 + 16384 + par * 2048;
#pragma unroll
          for (int i = 0; i < 10; ++i) { if (i < 8) AC16(xf[i], l16, ob + (i & 4) * 1024, (i & 3) * 1024); else AC16(xf[i], l16, ot, (i - 8) * 1024); } }
#pragma unroll
        for (int s_ = 0; s_ < 50; ++s_) { const int i = s_ / 5, nt = s_ % 5;
            if (nt == 0) WAITV(9 - i, "+v"(xf[i]));
            SB(); acc[nt] = mfma16(wr[s_ % WOD], xf[i], acc[nt]); SB();
            if (s_ + WOD < 50) wo_frag(s_ + WOD); }
        SB(); TIE("+v"(xres[0]), "+v"(xres[1]), "+v"(xres[2]), "+v"(b1v)); SB(); b1v |= b1v << 16;
        const u32 rb = L_WRED + w * 3072 + l16;
        STF4(rb, par ? acc[0] : acc[2]); STF4(rb + 1024, par ? acc[1] : acc[3]); STF4(rb + 2048, acc[4]);
        f32x4 ga[3], g2[3]; v2u g2p[3], sbp[3];
#pragma unroll
        for (int i = 0; i < 3; ++i) { const u32 co = (i < 2 ? par * 128 + i * 64 : 256) + g * 16;
            ga[i] = LDF4(L_OWN + co); g2[i] = LDF4(L_OWN + 320 + co); g2p[i] = LD8(L_OWN + 960 + co / 2); sbp[i] = LD8(L_OWN + 1120 + co / 2); }
        u32 xb = WS_XG + grp * 81920 + (mp >> 1) * 40960 + (16 * (mp & 1) + l15 + 32 * (g & 1)) * 16 + 5 * j * 1024, xb0 = xb + (2 * par + (g >> 1)) * 1024, xb1 = xb + 4096 + 8 * (g >> 1);
        u32 sto = WS_ST + (grp * 8 + j) * 4096 + (par * 3 + g) * 512 + (16 * mp + l15) * 8;
        f32x4 r[3]; r[0] = par ? acc[2] : acc[0]; r[1] = par ? acc[3] : acc[1]; r[2] = acc[4];
        TIE("+v"(xb0), "+v"(xb1), "+v"(sto), "+v"(r[0]), "+v"(r[1]));
        bar();
        TS(22);
        const u32 pb = L_WRED + (w ^ 1) * 3072 + l16;
        r[0] += LDF4(pb); r[1] += LDF4(pb + 1024); r[2] += LDF4(pb + 2048);
        u32 xg[6];
#pragma unroll
        for (int i = 0; i < 3; ++i) {
            x1p[2 * i] = pk2(r[i][0] * ga[i][0] + bflo(xres[i].x), r[i][1] * ga[i][1] + bfhi(xres[i].x)); x1p[2 * i + 1] = pk2(r[i][2] * ga[i][2] + bflo(xres[i].y), r[i][3] * ga[i][3] + bfhi(xres[i].y));
            xg[2 * i] = pk2(bflo(x1p[2 * i]) * g2[i][0], bfhi(x1p[2 * i]) * g2[i][1]); xg[2 * i + 1] = pk2(bflo(x1p[2 * i + 1]) * g2[i][2], bfhi(x1p[2 * i + 1]) * g2[i][3]);
        }
        CST16(xb0, ((v4u){xg[0], xg[1], xg[2], xg[3]}));
        if (hi == par) CST8(xb1, ((v2u){xg[4], xg[5]}));
        float sm[6] = {};
#pragma unroll
        for (int i = 0; i < 6; ++i) { const bool off = i >= 4 && hi != par; const u32 xa = off ? 0u : x1p[i], ya = off ? 0u : xg[i];
            sm[0] = dot2(xa, ONE2, sm[0]); sm[1] = dot2(xa, xa, sm[1]); sm[2] = dot2(ya, ONE2, sm[2]); sm[3] = dot2(ya, ya, sm[3]);
            sm[4] = dot2(ya, g2p[i >> 1][i & 1], sm[4]); sm[5] = dot2(ya, sbp[i >> 1][i & 1], sm[5]); }
        const float e0 = t32(sm[0], sm[4]), e1 = t32(sm[1], sm[5]), e2 = t32(sm[2], sm[2]), e3 = t32(sm[3], sm[3]);
        const float f0 = t16(e0, e2), f1 = t16(e1, e3);
        if (g < 3) CST8(sto, ((v2u){__float_as_uint(f0), __float_as_uint(f1)}));
        SB();
        WAITV(0);
        if ((lane & 7) == 0) DMA4(L_DUMMY, xb0, a.ws);
        if ((lane & 7) == 0) DMA4(L_DUMMY, (lane & 8) && g < 3 ? sto : (xb1 & ~255u) + hi * 128, a.ws);
    }
    bar();
    if (t == 0) __hip_atomic_store((GAS u32*)(ws + gfl + 32768 + j * 128), tag, RLX_AGENT);
    TS(14);
#pragma unroll
    for (int k = 32; k < 40; ++k) AC16(w1f[k], l16, w1o + (k & ~3) * 1024, (k & 3) * 1024);
    if (ua_own) {
        WAITV(0, W1T(32));
        ua_part(IC<32>{}, IC<40>{});
        if (!hi) {
#pragma unroll
            for (int i = 0; i < 3; ++i) __builtin_amdgcn_raw_buffer_store_b64(((v2u){pk2(up[i], up[i] - bfr(up[i])), tag}), rs, uav + i * 8, uao, 16); }
    }
    float cst[5];
    if (w == 7) {
#pragma unroll
        for (int i = 0; i < 5; ++i) cst[i] = 0;
#pragma unroll
        for (int r = 0; r < 2; ++r) { const int ch = lane + 64 * r; v4u gv = LD16(L_AUX + min(ch, 79) * 16), sv = LD16(L_AUX + 1280 + min(ch, 79) * 16);
#pragma unroll
            for (int e = 0; e < 4; ++e) { const u32 g_ = ch < 80 ? gv[e] : 0u, s_ = ch < 80 ? sv[e] : 0u;
                cst[0] = dot2(g_, ONE2, cst[0]); cst[1] = dot2(g_, g_, cst[1]); cst[2] = dot2(g_, s_, cst[2]); cst[3] = dot2(s_, ONE2, cst[3]); cst[4] = dot2(s_, s_, cst[4]); } }
#pragma unroll
        for (int i = 0; i < 5; ++i) { cst[i] = sum16(cst[i]); cst[i] = t16(cst[i], cst[i]); cst[i] = t32(cst[i], cst[i]); }
    }
    wait_flags(1, w, 1);
    TS(15);
    {
        v4u xi[13]; const u32 xb = WS_XG + grp * 81920 + 5 * w * 1024, sbo = WS_ST + (grp * 8 + w) * 4096;
#pragma unroll
        for (int i = 0; i < 3; ++i) AC16(xi[10 + i], l16, sbo, i * 1024);
#pragma unroll
        for (int i = 0; i < 10; ++i) AC16(xi[i], l16, xb + (i / 5) * 40960 + (i % 5 == 4 ? 4096 : 0), (i % 5 & 3) * 1024);
        WAITV(10, "+v"(xi[10]), "+v"(xi[11]), "+v"(xi[12]), W1T(32));
#pragma unroll
        for (int i = 0; i < 3; ++i) ST16(L_STP + w * 3072 + i * 1024 + l16, xi[10 + i]);
#pragma unroll
        for (int i = 0; i < 10; ++i) { WAITV(9 - i, "+v"(xi[i])); ST16(L_X + (i / 5) * 40960 + (5 * w + i % 5) * 1024 + l16, xi[i]); }
    }
    bar();
    TS(16);
    const int ks0 = w < 6 ? 7 * w : 42 + 6 * (w - 6), nks = w < 6 ? 7 : 6;
    v2u b2v[3];
    {
        const u32 w2o = WS_W2F + (5 * j * 54 + ks0) * 1024, w2l = WS_W2F + (5 * j * 54 + ks0 + nks - 1) * 1024;
        auto w2_load = [&](int k) { const int i = k / 5, nt = k % 5; if (i < 6) AC16R(w1f[k], l16, w2o + nt * 55296 + (i & ~3) * 1024, (i & 3) * 1024); else AC16R(w1f[k], l16, w2l + nt * 55296, 0); };
        f32x16 a0 = {}, a1 = {}; v4u ug0; v2u ug1;
        if (w == 7) {
            __builtin_amdgcn_s_setprio(3);
            f32x2 us[3] = {};
#pragma unroll
            for (int i = 0; i < 48; ++i) us[i % 3] += *(const LAS f32x2*)(lds + L_STP + i * 512 + lane * 8);
            const f32x4 u0 = {us[0].x, us[0].y, us[1].x, us[1].y}; const f32x2 u1 = us[2];
            const float in = 1.f / 640.f, m1 = u0.x * in, m1b = bfr(m1), s1 = ln_s(fmaxf(u0.y * in - m1 * m1, 0.f)), r1 = 1.f / s1;
            const float m2 = (r1 * (u0.z - m1b * cst[0]) + cst[3]) * in;
            const float q2 = (r1 * r1 * (u0.w - 2.f * m1b * u1.x + m1b * m1b * cst[1]) + 2.f * r1 * (u1.y - m1b * cst[2]) + cst[4]) * in;
            const float s2 = ln_s(fmaxf(q2 - m2 * m2, 0.f)), bw = -bfr(m2) * s1, gm = s1 * s2;
            const u32 bh = pk2(bw, gm), bl = pk2(bw - bflo(bh), gm - bfhi(bh));
            const u32 xe = L_XE + hi * 1024 + l31 * 16;
            ST16(xe, ((v4u){pk2(-m1b, -m1b), pk2(s1, s1), (bh & 0xffffu) | (bh << 16), bl & 0xffffu}));
            ST16(xe + 512, ((v4u){(bh >> 16) | (bl & 0xffff0000u), 0u, 0u, 0u}));
            STF(L_A + lane * 4, 1.f / gm);
            asm volatile("" ::: "memory"); STF(L_MRED, 1.f);
            __builtin_amdgcn_s_setprio(0);
        }
        {
            if (KPRIO && has_tile) __builtin_amdgcn_s_setprio(KPRIO);
            u32 xa = L_X + l16, xb2 = L_X + 40960 + l16; TIE("+v"(xb2));
            v4u x0[3], x1[3];
#pragma unroll
            for (int ks = 0; ks < 2; ++ks) { x0[ks] = LD16(xa + ks * 1024); x1[ks] = LD16(xb2 + ks * 1024); }
#pragma unroll
            for (int ks = 0; ks < 40; ++ks) {
                if (ks + 2 < 40) { x0[(ks + 2) % 3] = LD16(xa + (ks + 2) * 1024); x1[(ks + 2) % 3] = LD16(xb2 + (ks + 2) * 1024); }
                SB();
                a0 = mfma32(w1f[ks], x0[ks % 3], a0); a1 = mfma32(w1f[ks], x1[ks % 3], a1);
                SB();
                if (ks == UAK) { AC16(ug0, uav, uao, 0); AC8(ug1, uav, uao, 16); }
                if (ks >= 1 && ks < 36) w2_load(ks - 1); }
            if (KPRIO) __builtin_amdgcn_s_setprio(0);
            TIE("+v"(a0), "+v"(a1));
        }
        { const u32 bo = (80 * j + par * 32 + g * 4) * 2; AG8(b2v[0], bo, a.b2, 0); AG8(b2v[1], bo, a.b2, 32); AG8(b2v[2], (80 * j + 64 + g * 4) * 2, a.b2, 0); }
        TS(17);
        u32 hb = WS_H + grp * 221184 + ((l31 >> 4) * 54 + nt0 + min(w, ntile - 1)) * 1024 + (l15 + 16 * hi) * 16;
        WAITV(36 - UAK + 3, "+v"(ug0), "+v"(ug1));
        if (has_tile) {
            for (u32 sp = 0; sp < SPIN_CAP; ++sp) { if (__all(ug0.y == tag && ug0.w == tag && ug1.y == tag)) break; AC16(ug0, uav, uao, 0); AC8(ug1, uav, uao, 16); WAITV(0, "+v"(ug0), "+v"(ug1)); }
            const v4u wex = {hi ? b1v : ug0.x, hi ? 0u : ug0.z, hi ? 0u : ug1.x, hi ? 0u : (ug1.x & 0xffffu)};
            for (u32 sp = 0; sp < SPIN_CAP; ++sp) { if (*(volatile LAS float*)(lds + L_MRED) == 1.f) break; __builtin_amdgcn_s_sleep(1); }
            asm volatile("" ::: "memory");
            a0 = mfma32(wex, LD16(L_XE + l16), a0); a1 = mfma32(wex, LD16(L_XE + 1024 + l16), a1);
            const float A0 = LDF(L_A + l31 * 4), A1 = LDF(L_A + 128 + l31 * 4);
            const float k1 = -2.f * 0.7978845608028654f * 1.4426950408889634f, k3 = k1 * 0.044715f;
            auto gelu = [&](float z) { return z * __builtin_amdgcn_rcpf(1.f + __builtin_amdgcn_exp2f(z * (k1 + k3 * z * z))); };
#pragma unroll
            for (int tt = 0; tt < 2; ++tt)
#pragma unroll
                for (int hf = 0; hf < 2; ++hf) { v4u o; const float A = tt ? A1 : A0;
#pragma unroll
                    for (int e = 0; e < 4; ++e) { const int r = 8 * hf + 2 * e; o[e] = pk2(gelu(A * (tt ? a1[r] : a0[r])), gelu(A * (tt ? a1[r + 1] : a0[r + 1]))); }
                    CST16(hb + tt * 108 * 1024 + hf * 512, o); }
        }
        WAITV(0, W1T(0), W1T(8), "+v"(b2v[0]), "+v"(b2v[1]), "+v"(b2v[2])); TIE(W1T(16), W1T(24), "+v"(w1f[32]), "+v"(w1f[33]), "+v"(w1f[34]));
        if (has_tile && (lane & 7) < 4) DMA4(L_DUMMY, (hb & ~127u) + (lane & 1) * 512 + (lane & 2) * 54 * 1024, a.ws);
    }
    bar();
    if (t == 0) __hip_atomic_store((GAS u32*)(ws + gfl + 65536 + j * 128), tag, RLX_AGENT);
    TS(18);
    wait_flags(2, w, 1);
    TS(19);
    {
        f32x4 own[3]; v4u hf[14];
        const u32 hb = WS_H + grp * 221184 + ks0 * 1024;
        auto h_load = [&](int m, int i) { const u32 o = hb + ((mp + 1 + m) & 3) * 55296; v4u& d = hf[(m & 1) * 7 + i];
            if (i < 6) AC16(d, l16, o + (i & 4) * 1024, (i & 3) * 1024); else AC16(d, l16, o + (nks - 1) * 1024, 0); };
#pragma unroll
        for (int k = 0; k < 14; ++k) h_load(k / 7, k % 7);
#pragma unroll
        for (int m = 0; m < 4; ++m) { const int mt = (mp + 1 + m) & 3;
            f32x4 acc[5] = {};
#pragma unroll
            for (int i = 0; i < 7; ++i) { v4u& d = hf[(m & 1) * 7 + i];
                WAITV(m < 2 ? 13 : 27 - 7 * m - i, "+v"(d));
                if (i < 6 || nks == 7) {
#pragma unroll
                    for (int nt = 0; nt < 5; ++nt) acc[nt] = mfma16(w1f[i * 5 + nt], d, acc[nt]); }
                SB();
                if (m < 2) h_load(m + 2, i); }
            if (m == 3) {
                const u32 pb = L_PART + mt * 35840 + 6 * 5120 + l16;
                STF4(pb + (par ? 0 : 2048), par ? acc[0] : acc[2]); STF4(pb + (par ? 1024 : 3072), par ? acc[1] : acc[3]);
                if (hi != par) STF4(pb + 4096, acc[4]);
                own[0] = par ? acc[2] : acc[0]; own[1] = par ? acc[3] : acc[1]; own[2] = acc[4];
            } else {
                const u32 pb = L_PART + mt * 35840 + ((w - 2 * mt - 2) & 7) * 5120 + l16;
#pragma unroll
                for (int nt = 0; nt < 5; ++nt) STF4(pb + nt * 1024, acc[nt]);
            }
        }
        f32x4 gb[3], pr[3][7];
#pragma unroll
        for (int i = 0; i < 3; ++i) gb[i] = LDF4(L_OWN + 640 + ((i < 2 ? par * 32 + i * 16 : 64) + g * 4) * 4);
        bar();
        TS(20);
#pragma unroll
        for (int i = 0; i < 3; ++i)
#pragma unroll
            for (int k = 0; k < 7; ++k) pr[i][k] = LDF4(L_PART + mp * 35840 + (i < 2 ? 2 * par + i : 4) * 1024 + l16 + k * 5120);
        SB();
        GAS char* op = (GAS char*)a.out + (size_t)(row0 + 16 * mp + l15) * 1280 + j * 160 + g * 8;
        v2u ov[3];
#pragma unroll
        for (int i = 0; i < 3; ++i) {
            const f32x4 s = ((own[i] + pr[i][0]) + (pr[i][1] + pr[i][2])) + ((pr[i][3] + pr[i][4]) + (pr[i][5] + pr[i][6])); const v2u bb = b2v[i];
            const float o0 = bflo(x1p[2 * i]) + (s.x + bflo(bb.x)) * gb[i].x, o1 = bfhi(x1p[2 * i]) + (s.y + bfhi(bb.x)) * gb[i].y;
            const float o2 = bflo(x1p[2 * i + 1]) + (s.z + bflo(bb.y)) * gb[i].z, o3 = bfhi(x1p[2 * i + 1]) + (s.w + bfhi(bb.y)) * gb[i].w;
            ov[i] = (v2u){pk2(o0, o1), pk2(o2, o3)};
            if (i == 1) {
                const auto rx = __builtin_amdgcn_permlane16_swap(ov[0].x, ov[1].x, false, false), ry = __builtin_amdgcn_permlane16_swap(ov[0].y, ov[1].y, false, false);
                *(GAS v4u*)(op + par * 64 + (g & 1) * 24) = (v4u){rx[0], ry[0], rx[1], ry[1]}; }
        }
        if (hi == par) *(GAS v2u*)(op + 128) = ov[2];
    }
    }
    TS(21); TC(31);
    asm volatile(".Lfb_code_b:");
}

extern "C" void kernel_launch(void* const* d_in, const int* in_sizes, int n_in, void* d_out, int out_size, void* d_ws, size_t ws_size, hipStream_t stream) {
    Args a;
    a.x = (const char*)d_in[0]; a.wq = (const char*)d_in[1]; a.wk = (const char*)d_in[2]; a.wv = (const char*)d_in[3]; a.wo = (const char*)d_in[4];
    a.w1 = (const char*)d_in[6]; a.b1 = (const char*)d_in[7]; a.w2 = (const char*)d_in[8]; a.b2 = (const char*)d_in[9]; a.c = (const char*)d_in[10]; a.wmod = (const char*)d_in[11];
    a.out = (char*)d_out; a.ws = (char*)d_ws;
    (void)hipFuncSetAttribute((const void*)fused_block, hipFuncAttributeMaxDynamicSharedMemorySize, L_TOTAL);
    hipLaunchKernelGGL(fused_block, dim3(256), dim3(512), L_TOTAL, stream, a);
}
```
